```python
import torch

def _softmax(x, axis=-1):
    return torch.softmax(x, dim=axis)
def _gelu(x):
    return torch.nn.functional.gelu(x, approximate="tanh")
def _silu(x):
    return torch.nn.functional.silu(x)
def _sigmoid(x):
    return torch.sigmoid(x)
def _layernorm(x, axis=-1, eps=1e-5):
    m = x.mean(dim=axis, keepdim=True)
    v = x.var(dim=axis, keepdim=True, unbiased=False)
    return (x - m) / torch.sqrt(v + eps)
def _scatter_add(tgt, idx, src, axis=0):
    return tgt.clone().index_add_(axis, idx, src)
def _segment_sum(src, seg, n):
    out = torch.zeros((n,) + src.shape[1:], dtype=src.dtype)
    return out.index_add_(0, seg, src)
def _pad(x, pw):
    flat = tuple(v for pair in reversed(pw) for v in pair)
    return torch.nn.functional.pad(x, flat)

kernel_name = 'concat_heads__mha_block__ssm_conv_s4d__prompt'

def setup_inputs():

    _w = (lambda a: (lambda t: t.to(torch.bfloat16) if t.is_floating_point() else t)(torch.as_tensor(a)))
    def _A_setup():
        B, S, D, H = 1, 2048, 512, 32
        return {
            "x": _w((lambda *s: torch.randn(*s, dtype=torch.bfloat16) * 0.2)(B, S, D)),
            "wqkv": _w((lambda *s: torch.randn(*s, dtype=torch.bfloat16) * 0.2)(D, 3 * D)),
            "wo": _w((lambda *s: torch.randn(*s, dtype=torch.bfloat16) * 0.05)(D, D)),
        }
    _A_in = _A_setup()

    def _B_setup():
        Bsz, L, D, N = 1, 2048, 512, 128
        return {
            "u": _w((lambda *s: torch.randn(*s, dtype=torch.bfloat16) * 0.05)(Bsz, L, D)),
            "log_A_real": _w((lambda *s: torch.randn(*s, dtype=torch.bfloat16) * 6.4)(D, N) * 0.5),
            "A_imag": _w((lambda *s: torch.randn(*s, dtype=torch.bfloat16) * 0.05)(D, N) * 3.14),
            "C_real": _w((lambda *s: torch.randn(*s, dtype=torch.bfloat16) * 0.4)(D, N) / N ** 0.5),
            "C_imag": _w((lambda *s: torch.randn(*s, dtype=torch.bfloat16) * 0.4)(D, N) / N ** 0.5),
            "log_dt": _w((lambda *s: torch.randn(*s, dtype=torch.bfloat16) * 0.05)(D) * 0.5 - 4.0),
            "D_skip": _w((lambda *s: torch.randn(*s, dtype=torch.bfloat16) * 0.05)(D)),
        }
    _B_in = _B_setup()

    return {
        "A_x": _A_in["x"],
        "A_wqkv": _A_in["wqkv"],
        "A_wo": _A_in["wo"],
        "B_u": _B_in["u"],
        "B_log_A_real": _B_in["log_A_real"],
        "B_A_imag": _B_in["A_imag"],
        "B_C_real": _B_in["C_real"],
        "B_C_imag": _B_in["C_imag"],
        "B_log_dt": _B_in["log_dt"],
        "B_D_skip": _B_in["D_skip"],
    }

def reference(A_x, A_wqkv, A_wo, B_u, B_log_A_real, B_A_imag, B_C_real, B_C_imag, B_log_dt, B_D_skip):
    def _A_ref(x, wqkv, wo):
        B, S, D = x.shape
        H = 32
        Dh = D // H
        qkv = (lambda x, *d: x.permute(*d))((x @ wqkv).reshape(B, S, 3, H, Dh), 2, 0, 3, 1, 4)
        q, k, v = qkv[0], qkv[1], qkv[2]
        scores = torch.einsum("bhqd,bhkd->bhqk", q, k) * (Dh ** -0.5)
        p = _softmax(scores, axis=-1)
        o = torch.einsum("bhqk,bhkd->bhqd", p, v)
        return (lambda x, *d: x.permute(*d))(o, 0, 2, 1, 3).reshape(B, S, D) @ wo

    def _B_ref(u, log_A_real, A_imag, C_real, C_imag, log_dt, D_skip):
        L = u.shape[1]
        dt = torch.exp(log_dt)
        A = -torch.exp(log_A_real) + 1j * A_imag
        C = (C_real + 1j * C_imag) * dt[:, None]
        pos = (lambda x, dt: x.to(dt))(torch.arange(L), log_dt.dtype)
        P = torch.exp((A * dt[:, None])[:, :, None] * pos[None, None, :])
        K = 2.0 * torch.einsum("dn,dnl->dl", C, P).real
        L2 = 2 * L
        u_t = (lambda x, *d: x.permute(*d))(u, 0, 2, 1)
        Kf = (lambda x, *a, **k: torch.fft.rfft(x.float(), *a, **k))(K, L2)
        Uf = (lambda x, *a, **k: torch.fft.rfft(x.float(), *a, **k))(u_t, L2)
        y = (lambda x, *a, **k: torch.fft.irfft(x, *a, **k).to(torch.bfloat16))(Uf * Kf[None, :, :], L2)[:, :, :L]
        y = (lambda x, *d: x.permute(*d))(y, 0, 2, 1)
        return y + u * D_skip[None, None, :]

    a = _A_ref(A_x, A_wqkv, A_wo)
    b = _B_ref(B_u, B_log_A_real, B_A_imag, B_C_real, B_C_imag, B_log_dt, B_D_skip)
    return torch.concatenate([a, b], axis=-1)
```

```cpp
#include <hip/hip_runtime.h>
#include <hip/hip_bf16.h>
#include <stdint.h>

#define GAS __attribute__((address_space(1)))
#define LAS __attribute__((address_space(3)))
#define DEVI __device__ __forceinline__
typedef __bf16 bf16x2 __attribute__((ext_vector_type(2)));
typedef __bf16 bf16x8 __attribute__((ext_vector_type(8)));
typedef __bf16 bf16x16 __attribute__((ext_vector_type(16)));
typedef float f32x2 __attribute__((ext_vector_type(2)));
typedef float f32x4 __attribute__((ext_vector_type(4)));
typedef float f32x8 __attribute__((ext_vector_type(8)));
typedef float f32x16 __attribute__((ext_vector_type(16)));
typedef unsigned u32x2 __attribute__((ext_vector_type(2)));
typedef unsigned u32x4 __attribute__((ext_vector_type(4)));
typedef short s16x4 __attribute__((ext_vector_type(4)));
typedef short s16x8 __attribute__((ext_vector_type(8)));
typedef GAS unsigned long long gu64;
typedef LAS char* lptr;
typedef __amdgpu_buffer_rsrc_t rsrc_t;

constexpr unsigned WS_NONCE = 0;
constexpr unsigned WS_FLAG_KV = 4096;
constexpr unsigned WS_FLAG_O = WS_FLAG_KV + 256 * 128;
constexpr unsigned WS_KMAX = WS_FLAG_O + 2048;
constexpr unsigned WS_KV = 65536;
constexpr unsigned WS_O = WS_KV + 256 * 16384;
constexpr unsigned WS_USED = WS_O + 2048 * 512 * 2;
constexpr unsigned WS_DBG = 8u << 20;

constexpr int L_W = 0;
constexpr int L_XY = 0, XY_ROW = 528, XY_CH = 96 * XY_ROW;
constexpr int L_KD = 2 * XY_CH, KD_CH = 4112;
constexpr int L_P = 0, P_SZ = 2304;
constexpr int L_Q = 147456;
constexpr int L_MISC = 155648;
constexpr int L_KR = 0, KR_CP = 4160, KR_CH = 8 * KR_CP;
constexpr int L_U = 2 * KR_CH, U_CH = 4608;
constexpr int L_WO = L_U + 2 * U_CH;
constexpr int L_CP = 0;
constexpr int LDS_BYTES = 163840;
static_assert(L_KD + 2 * KD_CH <= L_Q && L_WO + 65536 <= L_Q && 64 * P_SZ <= L_Q, "LDS map");

constexpr float QSCALE = 0.25f * 1.4426950408889634f;
constexpr float SAFE2 = 60.f * 60.f;
constexpr unsigned SPIN_CAP = 1u << 18;
constexpr int AUX_SC1 = 16;

#define SBAR() __builtin_amdgcn_sched_barrier(0)
#define RLX_AGENT __ATOMIC_RELAXED, __HIP_MEMORY_SCOPE_AGENT

DEVI rsrc_t mk_rsrc(const void* p, unsigned bytes) { return __builtin_amdgcn_make_buffer_rsrc((void*)p, (short)0, (int)bytes, 0x00020000); }
DEVI u32x4 bld16(rsrc_t r, unsigned voff, unsigned soff) { return __builtin_amdgcn_raw_buffer_load_b128(r, voff, soff, 0); }
DEVI u32x4 bld16_sc1(rsrc_t r, unsigned voff, unsigned soff) { return __builtin_amdgcn_raw_buffer_load_b128(r, voff, soff, AUX_SC1); }
DEVI void bst16_sc1(u32x4 v, rsrc_t r, unsigned voff, unsigned soff) { __builtin_amdgcn_raw_buffer_store_b128(v, r, voff, soff, AUX_SC1); }
DEVI unsigned pk2(float lo, float hi) { f32x2 v = {lo, hi}; return __builtin_bit_cast(unsigned, __builtin_convertvector(v, bf16x2)); }
DEVI float bf_lo(unsigned v) { return __uint_as_float(v << 16); }
DEVI float bf_hi(unsigned v) { return __uint_as_float(v & 0xffff0000u); }
DEVI float bf2f(unsigned short v) { return __uint_as_float((unsigned)v << 16); }
DEVI float rbf(float x) { return bf_lo(pk2(x, 0.f)); }
DEVI bf16x8 asb(u32x4 v) { return __builtin_bit_cast(bf16x8, v); }
DEVI f32x4 mfma16(bf16x8 a, bf16x8 b, f32x4 c) { return __builtin_amdgcn_mfma_f32_16x16x32_bf16(a, b, c, 0, 0, 0); }
DEVI f32x16 mfma32(bf16x8 a, bf16x8 b, f32x16 c) { return __builtin_amdgcn_mfma_f32_32x32x16_bf16(a, b, c, 0, 0, 0); }
DEVI u32x4 lds16(lptr p) { return *(LAS u32x4*)p; }
DEVI void sts16(lptr p, u32x4 v) { *(LAS u32x4*)p = v; }
DEVI bf16x8 tr_frag(lptr p) {
    const s16x4 a = __builtin_amdgcn_ds_read_tr16_b64_v4i16((LAS s16x4*)p), b = __builtin_amdgcn_ds_read_tr16_b64_v4i16((LAS s16x4*)(p + 512));
    return __builtin_bit_cast(bf16x8, __builtin_shufflevector(a, b, 0, 1, 2, 3, 4, 5, 6, 7));
}
DEVI void swap16(unsigned& a, unsigned& b) { auto r = __builtin_amdgcn_permlane16_swap(a, b, false, false); a = r[0]; b = r[1]; }
DEVI void swap32(unsigned& a, unsigned& b) { auto r = __builtin_amdgcn_permlane32_swap(a, b, false, false); a = r[0]; b = r[1]; }
DEVI float sum_x16(float x) { unsigned a = __float_as_uint(x), b = a; swap16(a, b); return __uint_as_float(a) + __uint_as_float(b); }
DEVI float sum_x32(float x) { unsigned a = __float_as_uint(x), b = a; swap32(a, b); return __uint_as_float(a) + __uint_as_float(b); }
DEVI float max_x16(float x) { unsigned a = __float_as_uint(x), b = a; swap16(a, b); return fmaxf(__uint_as_float(a), __uint_as_float(b)); }
DEVI float max_x32(float x) { unsigned a = __float_as_uint(x), b = a; swap32(a, b); return fmaxf(__uint_as_float(a), __uint_as_float(b)); }
template <int C> DEVI float dppf(float x) { return __int_as_float(__builtin_amdgcn_mov_dpp(__float_as_int(x), C, 0xf, 0xf, true)); }
DEVI float nanmax(float a, float b) { return (a > b || a != a) ? a : b; }
DEVI float row_nanmax(float x) { x = nanmax(x, dppf<0x128>(x)); x = nanmax(x, dppf<0x124>(x)); x = nanmax(x, dppf<0x122>(x)); return nanmax(x, dppf<0x121>(x)); }
DEVI float row_sum(float x) { x += dppf<0x128>(x); x += dppf<0x124>(x); x += dppf<0x122>(x); return x + dppf<0x121>(x); }
DEVI void wg_barrier() { asm volatile("s_waitcnt lgkmcnt(0)\n\ts_barrier" ::: "memory"); }
DEVI void drain_vm() { asm volatile("s_waitcnt vmcnt(0)" ::: "memory"); }
DEVI unsigned long long mix64(unsigned long long z) {
    z += 0x9E3779B97F4A7C15ull; z = (z ^ (z >> 30)) * 0xBF58476D1CE4E5B9ull; z = (z ^ (z >> 27)) * 0x94D049BB133111EBull; return z ^ (z >> 31); }
DEVI void wait_tag(gu64* word, unsigned long long tag) {
    for (unsigned spins = 0; spins < SPIN_CAP; ++spins) {
        const unsigned long long v = __hip_atomic_load(word, RLX_AGENT);
        if (__all(v == tag)) break;
        __builtin_amdgcn_s_sleep(2);
    }
    asm volatile("" ::: "memory");
}
#define STAMP(i) do {} while (0)

template <bool SHIFT>
DEVI void attn_block(const bf16x8 (&kf)[8], const bf16x8 (&vf)[8][2], bf16x8 qf, f32x16& o, float& m_out) {
    const int idx = 0xDDDD;
    f32x16 c = {};
    float m = 0.f;
    if (SHIFT) {
        m = -INFINITY;
#pragma unroll
        for (int t = 0; t < 8; ++t) { const f32x16 s = mfma32(kf[t], qf, c);
#pragma unroll
            for (int r = 0; r < 16; ++r) m = fmaxf(m, s[r]); }
        m = max_x32(m);
#pragma unroll
        for (int r = 0; r < 16; ++r) c[r] = -m;
    }
    o = f32x16{};
#pragma unroll
    for (int t = 0; t < 8; ++t) {
        f32x16 s = mfma32(kf[t], qf, c);
#pragma unroll
        for (int r = 0; r < 16; ++r) s[r] = __builtin_amdgcn_exp2f(s[r]);
        const f32x8 lo = __builtin_shufflevector(s, s, 0, 1, 2, 3, 4, 5, 6, 7), hi = __builtin_shufflevector(s, s, 8, 9, 10, 11, 12, 13, 14, 15);
        o = __builtin_amdgcn_smfmac_f32_32x32x32_bf16(vf[t][0], __builtin_bit_cast(bf16x16, lo), o, idx, 0, 0);
        o = __builtin_amdgcn_smfmac_f32_32x32x32_bf16(vf[t][1], __builtin_bit_cast(bf16x16, hi), o, idx, 0, 0);
    }
    m_out = m;
}

__global__ __launch_bounds__(512, 2) void fused_kernel(
    const unsigned short* __restrict__ x, const unsigned short* __restrict__ wqkv, const unsigned short* __restrict__ wo,
    const unsigned short* __restrict__ u, const unsigned short* __restrict__ lar, const unsigned short* __restrict__ aim,
    const unsigned short* __restrict__ cre, const unsigned short* __restrict__ cim, const unsigned short* __restrict__ ldt,
    const unsigned short* __restrict__ dsk, unsigned short* __restrict__ out, char* __restrict__ ws_) {
    extern __shared__ __attribute__((aligned(16))) char smem_[];
    const lptr lds = (lptr)smem_;
    GAS char* ws = (GAS char*)ws_;
    const int tid = threadIdx.x, lane = tid & 63, w = __builtin_amdgcn_readfirstlane(tid >> 6);
    const int r16 = lane & 15, g = lane >> 4, l31 = lane & 31, hh = lane >> 5;
    const int xcd = blockIdx.x & 7, sl = blockIdx.x >> 3, h = 4 * xcd + (sl >> 3), j = sl & 7, d0 = 64 * xcd + 2 * sl;
    const rsrc_t rws = mk_rsrc(ws_, WS_USED);
    STAMP(0);
    const unsigned long long tag = mix64(__hip_atomic_load((gu64*)(ws + WS_NONCE), RLX_AGENT));

    unsigned uu[4];
    {
        const rsrc_t rw = mk_rsrc(wqkv, 512 * 1536 * 2), rx = mk_rsrc(x, 2048 * 512 * 2);
        u32x4 wr[6];
        const unsigned wv = ((8 * ((lane >> 3) & 3) + 4 * hh + ((lane >> 1) & 3)) * 1536 + 8 * (lane & 1)) * 2;
#pragma unroll
        for (int i = 0; i < 6; ++i) { const int f = w * 6 + i, ks = f / 3, p = f - 3 * ks; wr[i] = bld16(rw, wv, (32 * ks * 1536 + p * 512 + 16 * h) * 2); }
        u32x4 xf[16][2];
        const unsigned xv = (r16 * 512 + 8 * g) * 2, xs = (256 * j + 32 * w) * 1024;
#pragma unroll
        for (int ks = 0; ks < 16; ++ks) { xf[ks][0] = bld16(rx, xv + 64 * ks, xs); xf[ks][1] = bld16(rx, xv + 64 * ks, xs + 16384); }
        if (tid == 0) *(LAS unsigned*)(lds + L_MISC + 32) = 0u;
#pragma unroll
        for (int i = 0; i < 6; ++i) sts16(lds + L_W + (w * 6 + i) * 1024 + lane * 16, wr[i]);
        wg_barrier();
        STAMP(1);
        f32x4 aq[2] = {}, ak[2] = {}, av[2] = {};
#pragma unroll
        for (int ks = 0; ks < 16; ++ks) {
            const lptr wp = lds + L_W + ks * 3072 + lane * 8;
            const bf16x8 fq = tr_frag(wp), fk = tr_frag(wp + 1024), fv = tr_frag(wp + 2048);
#pragma unroll
            for (int tb = 0; tb < 2; ++tb) { const bf16x8 xb = asb(xf[ks][tb]);
                aq[tb] = mfma16(fq, xb, aq[tb]); ak[tb] = mfma16(fk, xb, ak[tb]); av[tb] = mfma16(xb, fv, av[tb]); }
        }
        STAMP(2);
#pragma unroll
        for (int i = 0; i < 4; ++i) uu[i] = *(const unsigned*)(u + (4 * tid + i) * 512 + d0);
        float q2 = 0.f, k2 = 0.f;
        u32x4 qw, kw, vw;
#pragma unroll
        for (int tb = 0; tb < 2; ++tb) {
            const f32x4 q = aq[tb] * QSCALE, k = ak[tb], v = av[tb];
            q2 = nanmax(q2, sum_x32(sum_x16(q[0] * q[0] + q[1] * q[1] + q[2] * q[2] + q[3] * q[3])));
            k2 = nanmax(k2, sum_x32(sum_x16(k[0] * k[0] + k[1] * k[1] + k[2] * k[2] + k[3] * k[3])));
            qw[2 * tb] = pk2(q[0], q[1]); qw[2 * tb + 1] = pk2(q[2], q[3]);
            kw[2 * tb] = pk2(k[0], k[1]); kw[2 * tb + 1] = pk2(k[2], k[3]);
            vw[2 * tb] = pk2(v[0], v[1]); vw[2 * tb + 1] = pk2(v[2], v[3]);
        }
        auto sw = [](u32x4 a) { unsigned a0 = a[0], a1 = a[1], b0 = a[2], b1 = a[3]; swap16(a0, b0); swap16(a1, b1); return (u32x4){a0, a1, b0, b1}; };
        qw = sw(qw); kw = sw(kw); vw = sw(vw);
        q2 = row_nanmax(q2); k2 = row_nanmax(k2);
        const unsigned kvb = WS_KV + (h * 8 + j) * 16384 + w * 2048;
        const unsigned kpos = (g >> 1) * 512 + (16 * (g & 1) + r16) * 16;
        bst16_sc1(kw, rws, kpos, kvb);
        bst16_sc1(vw, rws, (g & 1) * 512 + (g >> 1) * 256 + r16 * 16, kvb + 1024);
        if (lane == 0) { __hip_atomic_store((GAS unsigned*)(ws + WS_KMAX) + (h * 8 + j) * 8 + w, __float_as_uint(k2), RLX_AGENT);
                         *(LAS float*)(lds + L_MISC + 4 * w) = q2; }
        sts16(lds + L_Q + w * 1024 + kpos, qw);
        drain_vm();
        wg_barrier();
        if (tid == 0) __hip_atomic_store((gu64*)(ws + WS_FLAG_KV + (h * 8 + j) * 128), tag, RLX_AGENT);
        STAMP(3);
    }

    {
        const int role = w >> 2, ch = (w >> 1) & 1, n = tid & 127, d = d0 + ch;
        const float dt = rbf(expf(bf2f(ldt[d])));
        const float zr = -rbf(expf(bf2f(lar[d * 128 + n]))) * dt, zi = bf2f(aim[d * 128 + n]) * dt;
        const float cr = 2.f * bf2f(cre[d * 128 + n]) * dt, ci = 2.f * bf2f(cim[d * 128 + n]) * dt;
        const float zl = zr * 1.4426950408889634f, zt = zi * 0.15915494309189535f;
        auto cexp = [&](float p, float& er, float& ei) { const float mag = __builtin_amdgcn_exp2f(zl * p), a = __builtin_amdgcn_fractf(zt * p);
            er = mag * __builtin_amdgcn_cosf(a); ei = mag * __builtin_amdgcn_sinf(a); };
        float er, ei; cexp(32.f, er, ei);
        const lptr xp = lds + L_XY + ch * XY_CH + n * 4;
        auto step = [](float& xr, float& xi, float fr, float fi) { const float t = xr * fr - xi * fi; xi = xr * fi + xi * fr; xr = t; };
        if (role == 0) {
            float xr = cr, xi = ci;
#pragma unroll
            for (int i = 0; i < 48; ++i) { *(LAS unsigned*)(xp + i * XY_ROW) = pk2(xr, xi); step(xr, xi, er, ei); }
        } else {
            float sr, si; cexp(1536.f, sr, si);
            float xr = cr, xi = ci; step(xr, xi, sr, si);
#pragma unroll
            for (int i = 48; i < 64; ++i) { *(LAS unsigned*)(xp + i * XY_ROW) = pk2(xr, xi); step(xr, xi, er, ei); }
            float s = sum_x32(sum_x16(row_sum(xr)));
            if (lane == 0) *(LAS float*)(lds + L_MISC + 48 + 4 * (w & 3)) = s;
            float fr, fi; cexp(1.f, fr, fi);
            float yr = 1.f, yi = 0.f;
#pragma unroll
            for (int i = 0; i < 32; ++i) { *(LAS unsigned*)(xp + (64 + i) * XY_ROW) = pk2(yr, -yi); step(yr, yi, fr, fi); }
        }
        wg_barrier();
        STAMP(4);
    }

    bf16x8 kf[8], vf[8][2];
    float k2w;
    {
        wait_tag((gu64*)(ws + WS_FLAG_KV + (h * 8 + w) * 128), tag);
        STAMP(5);
        const unsigned kvb = WS_KV + (h * 8 + w) * 16384;
        const unsigned vpos = ((lane >> 4) & 1) * 512 + hh * 256 + r16 * 16;
        u32x4 vr[8];
#pragma unroll
        for (int t = 0; t < 8; ++t) { kf[t] = asb(bld16_sc1(rws, lane * 16 + t * 2048, kvb)); vr[t] = bld16_sc1(rws, vpos + 1024 + t * 2048, kvb); }
        k2w = __uint_as_float(__builtin_amdgcn_raw_buffer_load_b32(rws, (lane & 7) * 4, WS_KMAX + (h * 8 + w) * 32, AUX_SC1));

        {
            const int ch = w >> 2, mb = w & 3;
            const lptr ap = lds + L_XY + ch * XY_CH + (16 * mb + r16) * XY_ROW + g * 16;
            const lptr bp = lds + L_XY + ch * XY_CH + (64 + r16) * XY_ROW + g * 16;
            f32x4 a0 = {}, a1 = {};
#pragma unroll
            for (int ks = 0; ks < 8; ++ks) { const bf16x8 a = asb(lds16(ap + ks * 64));
                a0 = mfma16(a, asb(lds16(bp + ks * 64)), a0); a1 = mfma16(a, asb(lds16(bp + 16 * XY_ROW + ks * 64)), a1); }
            const lptr kp = lds + L_KD + ch * KD_CH + ((16 * mb + 4 * g) * 32 + r16) * 2;
#pragma unroll
            for (int i = 0; i < 4; ++i) { const unsigned v = pk2(a0[i], a1[i]);
                *(LAS unsigned short*)(kp + i * 64) = (unsigned short)v; *(LAS unsigned short*)(kp + i * 64 + 32) = (unsigned short)(v >> 16); }
            if (lane == 0 && mb == 0) { const float s = *(LAS float*)(lds + L_MISC + 48 + 8 * ch) + *(LAS float*)(lds + L_MISC + 52 + 8 * ch);
                *(LAS unsigned short*)(lds + L_KD + ch * KD_CH + 4096) = (unsigned short)pk2(s, 0.f); }
        }
        wg_barrier();
#pragma unroll
        for (int t = 0; t < 8; ++t) {
            u32x4 one = {0x3F803F80u, 0x3F803F80u, 0x3F803F80u, 0x3F803F80u};
            unsigned a[4], b[4];
#pragma unroll
            for (int i = 0; i < 4; ++i) { a[i] = vr[t][i]; b[i] = one[i]; swap16(a[i], b[i]); }
            vf[t][0] = asb((u32x4){a[0], a[1], a[2], a[3]}); vf[t][1] = asb((u32x4){b[0], b[1], b[2], b[3]});
        }
    }
    unsigned kr[8];
    {
        const int ch = tid >> 8, y = tid & 255;
        const lptr kp = lds + L_KD + ch * KD_CH;
#pragma unroll
        for (int i = 0; i < 8; ++i) {
            const int l0 = 2047 - 8 * y - 2 * i, l1 = l0 - 1;
            const unsigned pp = pk2((float)l0, (float)l1);
            const int p0 = (int)bf_lo(pp), p1 = (int)bf_hi(pp);
            const unsigned v0 = l0 >= 0 ? *(LAS unsigned short*)(kp + 2 * max(p0, 0)) : 0u, v1 = l1 >= 0 ? *(LAS unsigned short*)(kp + 2 * max(p1, 0)) : 0u;
            kr[i] = v0 | (v1 << 16);
        }
        wg_barrier();
        STAMP(6);
    }

    {
        float q2 = *(LAS float*)(lds + L_MISC + 4 * (lane & 7));
        q2 = nanmax(q2, dppf<0x124>(q2)); q2 = nanmax(q2, dppf<0x122>(q2)); q2 = nanmax(q2, dppf<0x121>(q2));
        k2w = nanmax(k2w, dppf<0x124>(k2w)); k2w = nanmax(k2w, dppf<0x122>(k2w)); k2w = nanmax(k2w, dppf<0x121>(k2w));
        const bool fast = __builtin_amdgcn_readfirstlane((q2 * k2w * 1.05f <= SAFE2) ? 1 : 0) != 0;
        const lptr qp = lds + L_Q + lane * 16;
        const lptr pp = lds + L_P + w * P_SZ;
        if (fast) {
#pragma unroll 1
            for (int qb = 0; qb < 8; ++qb) {
                f32x16 o; float m;
                attn_block<false>(kf, vf, asb(lds16(qp + qb * 1024)), o, m);
                const lptr p = pp + qb * (8 * P_SZ);
                *(LAS f32x4*)(p + lane * 16) = (f32x4){o[0], o[1], o[2], o[3]};
                *(LAS f32x4*)(p + 1024 + lane * 16) = (f32x4){o[4], o[5], o[6], o[7]};
                *(LAS float*)(p + 2048 + lane * 4) = hh ? 0.f : o[8];
            }
        } else {
            if (lane == 0) *(LAS unsigned*)(lds + L_MISC + 32) = 1u;
#pragma unroll 1
            for (int qb = 0; qb < 8; ++qb) {
                f32x16 o; float m;
                attn_block<true>(kf, vf, asb(lds16(qp + qb * 1024)), o, m);
                const lptr p = pp + qb * (8 * P_SZ);
                *(LAS f32x4*)(p + lane * 16) = (f32x4){o[0], o[1], o[2], o[3]};
                *(LAS f32x4*)(p + 1024 + lane * 16) = (f32x4){o[4], o[5], o[6], o[7]};
                *(LAS float*)(p + 2048 + lane * 4) = hh ? m : o[8];
            }
        }
        STAMP(7);
        wg_barrier();
        STAMP(8);
        const lptr p = lds + L_P + w * (8 * P_SZ);
        f32x4 oa = {}, ob = {}; float l = 0.f;
        if (*(LAS unsigned*)(lds + L_MISC + 32) == 0u) {
#pragma unroll
            for (int i = 0; i < 8; ++i) { oa += *(LAS f32x4*)(p + i * P_SZ + lane * 16); ob += *(LAS f32x4*)(p + i * P_SZ + 1024 + lane * 16);
                l += *(LAS float*)(p + i * P_SZ + 2048 + l31 * 4); }
        } else {
            float mi[8], mx = -INFINITY;
#pragma unroll
            for (int i = 0; i < 8; ++i) { mi[i] = *(LAS float*)(p + i * P_SZ + 2048 + 128 + l31 * 4); mx = fmaxf(mx, mi[i]); }
#pragma unroll
            for (int i = 0; i < 8; ++i) { const float f = __builtin_amdgcn_exp2f(mi[i] - mx);
                oa += *(LAS f32x4*)(p + i * P_SZ + lane * 16) * f; ob += *(LAS f32x4*)(p + i * P_SZ + 1024 + lane * 16) * f;
                l += *(LAS float*)(p + i * P_SZ + 2048 + l31 * 4) * f; }
        }
        const float rl = 1.f / l;
        oa *= rl; ob *= rl;
        unsigned a0 = pk2(oa[0], oa[1]), a1 = pk2(oa[2], oa[3]), b0 = pk2(ob[0], ob[1]), b1 = pk2(ob[2], ob[3]);
        swap32(a0, b0); swap32(a1, b1);
        const u32x4 ow = {a0, a1, b0, b1};
        bst16_sc1(ow, rws, (l31 >> 4) * 16384 + (l31 & 15) * 32 + hh * 16, WS_O + (16 * j + 2 * w) * 16384 + h * 512);
        wg_barrier();
    }

    {
        const int ch = tid >> 8, y = tid & 255;
        unsigned sft[7];
#pragma unroll
        for (int i = 0; i < 7; ++i) sft[i] = __builtin_amdgcn_alignbit(kr[i + 1], kr[i], 16);
        const lptr kp = lds + L_KR + ch * KR_CH + y * 16;
#pragma unroll
        for (int c = 0; c < 4; ++c) {
            sts16(kp + (2 * c) * KR_CP, (u32x4){kr[c], kr[c + 1], kr[c + 2], kr[c + 3]});
            sts16(kp + (2 * c + 1) * KR_CP, (u32x4){sft[c], sft[c + 1], sft[c + 2], sft[c + 3]});
        }
        if (tid < 16) sts16(lds + L_KR + (tid >> 3) * KR_CH + (tid & 7) * KR_CP + 4096, (u32x4){0u, 0u, 0u, 0u});
        if (tid < 64) sts16(lds + L_U + (tid >> 5) * U_CH + (tid & 31) * 16, (u32x4){0u, 0u, 0u, 0u});
        const unsigned sel_lo = 0x05040100u, sel_hi = 0x07060302u;
        *(LAS u32x2*)(lds + L_U + 512 + tid * 8) = (u32x2){__builtin_amdgcn_perm(uu[1], uu[0], sel_lo), __builtin_amdgcn_perm(uu[3], uu[2], sel_lo)};
        *(LAS u32x2*)(lds + L_U + U_CH + 512 + tid * 8) = (u32x2){__builtin_amdgcn_perm(uu[1], uu[0], sel_hi), __builtin_amdgcn_perm(uu[3], uu[2], sel_hi)};
        drain_vm();
        wg_barrier();
        if (tid == 0) __hip_atomic_store((gu64*)(ws + WS_FLAG_O + (j * 32 + h) * 8), tag, RLX_AGENT);
        STAMP(9);
    }

    {
        const int ncol0 = 256 * (xcd >> 2) + 64 * (h & 3);
        const rsrc_t rwo = mk_rsrc(wo, 512 * 512 * 2);
        u32x4 wr[8];
        const unsigned wv = ((8 * ((lane >> 3) & 3) + 4 * hh + ((lane >> 1) & 3)) * 512 + 8 * (lane & 1)) * 2;
#pragma unroll
        for (int i = 0; i < 8; ++i) wr[i] = bld16(rwo, wv + (i & 3) * 32, (32 * (2 * w + (i >> 2)) * 512 + ncol0) * 2);

        const int ch = w >> 2, cs = (w >> 1) & 1, rg = w & 1;
        const lptr ab = lds + L_KR + ch * KR_CH + (7 - (r16 & 7)) * KR_CP + 2 * (2040 - 8 * (r16 >> 3) - 16 * (g >> 1) + 8 * (g & 1));
        const lptr bb = lds + L_U + ch * U_CH + 512 + 32 * (r16 - (g >> 1)) + 16 * (g & 1);
        f32x4 acc[4] = {};
        auto conv = [&](auto CS) {
            constexpr int cs_ = decltype(CS)::value;
            constexpr int C0 = cs_ ? 1 : 0, C1 = cs_ ? 2 : 3, C2 = cs_ ? 5 : 4, C3 = cs_ ? 6 : 7;
#pragma unroll 1
            for (int rr = 0; rr < 4; ++rr) {
                const int r = 4 * rg + rr;
                const lptr ap = ab - 64 * r - 3584, bp = bb - 64 * r;
                bf16x8 A[8], B[8];
#pragma unroll
                for (int m = 0; m <= C3; ++m) { A[m] = asb(lds16(ap + 512 * (7 - m))); B[m] = asb(lds16(bp + 512 * m)); }
#pragma unroll
                for (int m = 0; m <= C3; ++m) {
                    if (m <= C0) acc[0] = mfma16(A[m], B[C0 - m < 0 ? 0 : C0 - m], acc[0]);
                    if (m <= C1) acc[1] = mfma16(A[m], B[C1 - m < 0 ? 0 : C1 - m], acc[1]);
                    if (m <= C2) acc[2] = mfma16(A[m], B[C2 - m < 0 ? 0 : C2 - m], acc[2]);
                    acc[3] = mfma16(A[m], B[C3 - m], acc[3]);
                }
            }
        };
        if (cs) conv(std::integral_constant<int, 1>{}); else conv(std::integral_constant<int, 0>{});
#pragma unroll
        for (int i = 0; i < 8; ++i) sts16(lds + L_WO + ((2 * w + (i >> 2)) * 4 + (i & 3)) * 1024 + lane * 16, wr[i]);
        STAMP(10);
        wg_barrier();
        {
            const lptr cp = lds + L_CP + (ch * 2 + rg) * 8192 + (4 * r16 + g) * 16;
            const int c0 = cs ? 1 : 0, c1 = cs ? 2 : 3, c2 = cs ? 5 : 4, c3 = cs ? 6 : 7;
            *(LAS f32x4*)(cp + c0 * 1024) = acc[0]; *(LAS f32x4*)(cp + c1 * 1024) = acc[1];
            *(LAS f32x4*)(cp + c2 * 1024) = acc[2]; *(LAS f32x4*)(cp + c3 * 1024) = acc[3];
        }
        if (w == 0) wait_tag((gu64*)(ws + WS_FLAG_O + (j * 32 + l31) * 8), tag);
        wg_barrier();
        STAMP(11);
        if (blockIdx.x == 0 && tid == 0) __hip_atomic_store((gu64*)(ws + WS_NONCE), tag, RLX_AGENT);
        const int rgp = w & 3, c2h = w >> 2, row0 = 256 * j + 64 * (xcd & 3) + 16 * rgp;
        u32x4 of[16];
        const unsigned ov = (g >> 1) * 512 + r16 * 32 + (g & 1) * 16;
#pragma unroll
        for (int ks = 0; ks < 16; ++ks) of[ks] = bld16_sc1(rws, ov + ks * 1024, WS_O + (row0 >> 4) * 16384);
        {
            const lptr cp = lds + L_CP + tid * 16;
            const f32x4 y0 = *(LAS f32x4*)cp + *(LAS f32x4*)(cp + 8192), y1 = *(LAS f32x4*)(cp + 16384) + *(LAS f32x4*)(cp + 24576);
            const unsigned dd = *(const unsigned*)(dsk + d0);
            const float s0 = bf_lo(dd), s1 = bf_hi(dd);
#pragma unroll
            for (int i = 0; i < 4; ++i) {
                const unsigned yy = pk2(y0[i], y1[i]), us = pk2(bf_lo(uu[i]) * s0, bf_hi(uu[i]) * s1);
                *(unsigned*)(out + (4 * tid + i) * 1024 + 512 + d0) = pk2(bf_lo(yy) + bf_lo(us), bf_hi(yy) + bf_hi(us));
            }
        }
        f32x4 p0 = {}, p1 = {};
        const lptr wp = lds + L_WO + (2 * c2h) * 1024 + lane * 8;
#pragma unroll
        for (int ks = 0; ks < 16; ++ks) { const bf16x8 b = asb(of[ks]);
            p0 = mfma16(tr_frag(wp + ks * 4096), b, p0); p1 = mfma16(tr_frag(wp + ks * 4096 + 1024), b, p1); }
        unsigned a0 = pk2(p0[0], p0[1]), a1 = pk2(p0[2], p0[3]), b0 = pk2(p1[0], p1[1]), b1 = pk2(p1[2], p1[3]);
        swap16(a0, b0); swap16(a1, b1);
        *(u32x4*)(out + (row0 + r16) * 1024 + ncol0 + 32 * c2h + 16 * (g & 1) + 8 * (g >> 1)) = (u32x4){a0, a1, b0, b1};
        STAMP(12);
    }
}

extern "C" void kernel_launch(void* const* d_in, const int* in_sizes, int n_in, void* d_out, int out_size, void* d_ws, size_t ws_size, hipStream_t stream) {
    (void)in_sizes; (void)n_in; (void)out_size; (void)ws_size;
    (void)hipFuncSetAttribute((const void*)fused_kernel, hipFuncAttributeMaxDynamicSharedMemorySize, LDS_BYTES);
    typedef const unsigned short* cp;
    fused_kernel<<<256, 512, LDS_BYTES, stream>>>((cp)d_in[0], (cp)d_in[1], (cp)d_in[2], (cp)d_in[3], (cp)d_in[4], (cp)d_in[5], (cp)d_in[6], (cp)d_in[7],
                                                  (cp)d_in[8], (cp)d_in[9], (unsigned short*)d_out, (char*)d_ws);
}
```

```cpp
#include <hip/hip_runtime.h>
#include <hip/hip_bf16.h>
#include <stdint.h>

#define GAS __attribute__((address_space(1)))
#define LAS __attribute__((address_space(3)))
#define DEVI __device__ __forceinline__
typedef __bf16 bf16x2 __attribute__((ext_vector_type(2)));
typedef __bf16 bf16x8 __attribute__((ext_vector_type(8)));
typedef __bf16 bf16x16 __attribute__((ext_vector_type(16)));
typedef float f32x2 __attribute__((ext_vector_type(2)));
typedef float f32x4 __attribute__((ext_vector_type(4)));
typedef float f32x8 __attribute__((ext_vector_type(8)));
typedef float f32x16 __attribute__((ext_vector_type(16)));
typedef unsigned u32x2 __attribute__((ext_vector_type(2)));
typedef unsigned u32x4 __attribute__((ext_vector_type(4)));
typedef short s16x4 __attribute__((ext_vector_type(4)));
typedef short s16x8 __attribute__((ext_vector_type(8)));
typedef GAS unsigned long long gu64;
typedef LAS char* lptr;
typedef __amdgpu_buffer_rsrc_t rsrc_t;

constexpr unsigned WS_NONCE = 0;
constexpr unsigned WS_FLAG_P = 4096;
constexpr unsigned WS_FLAG_O = WS_FLAG_P + 256 * 128;
constexpr unsigned WS_NRM = WS_FLAG_O + 2048;
constexpr unsigned WS_QKV = 65536;
constexpr unsigned WS_O = WS_QKV + 32 * 64 * 3072;
constexpr unsigned WS_USED = WS_O + 2048 * 512 * 2;
constexpr unsigned WS_DBG = 16u << 20;

constexpr int L_RING = 0, SLOT = 32768;
constexpr int L_XY = 0, XY_ROW = 528, XY_CH = 96 * XY_ROW;
constexpr int L_KD = 2 * XY_CH, KD_CH = 4112;
constexpr int L_P = 0, P_SZ = 2304;
constexpr int L_Q = 147456;
constexpr int L_MISC = 155648;
constexpr int L_KR = 0, KR_CP = 4160, KR_CH = 8 * KR_CP;
constexpr int L_U = 2 * KR_CH, U_CH = 4608;
constexpr int L_WO = L_U + 2 * U_CH;
constexpr int L_CP = 0;
constexpr int LDS_BYTES = 163840;
static_assert(L_KD + 2 * KD_CH <= L_Q && L_WO + 65536 <= L_Q && 64 * P_SZ <= L_Q, "LDS map");

constexpr float QSCALE = 0.25f * 1.4426950408889634f;
constexpr float SAFE2 = 60.f * 60.f;
constexpr unsigned SPIN_CAP = 1u << 18;
constexpr int AUX_SC1 = 16;

#define SBAR() __builtin_amdgcn_sched_barrier(0)
#define RLX_AGENT __ATOMIC_RELAXED, __HIP_MEMORY_SCOPE_AGENT

DEVI rsrc_t mk_rsrc(const void* p, unsigned bytes) { return __builtin_amdgcn_make_buffer_rsrc((void*)p, (short)0, (int)bytes, 0x00020000); }
DEVI u32x4 bld16(rsrc_t r, unsigned voff, unsigned soff) { return __builtin_amdgcn_raw_buffer_load_b128(r, voff, soff, 0); }
DEVI u32x4 bld16_sc1(rsrc_t r, unsigned voff, unsigned soff) { return __builtin_amdgcn_raw_buffer_load_b128(r, voff, soff, AUX_SC1); }
DEVI void bst16_sc1(u32x4 v, rsrc_t r, unsigned voff, unsigned soff) { __builtin_amdgcn_raw_buffer_store_b128(v, r, voff, soff, AUX_SC1); }
DEVI unsigned pk2(float lo, float hi) { f32x2 v = {lo, hi}; return __builtin_bit_cast(unsigned, __builtin_convertvector(v, bf16x2)); }
DEVI float bf_lo(unsigned v) { return __uint_as_float(v << 16); }
DEVI float bf_hi(unsigned v) { return __uint_as_float(v & 0xffff0000u); }
DEVI float bf2f(unsigned short v) { return __uint_as_float((unsigned)v << 16); }
DEVI float rbf(float x) { return bf_lo(pk2(x, 0.f)); }
DEVI bf16x8 asb(u32x4 v) { return __builtin_bit_cast(bf16x8, v); }
DEVI f32x4 mfma16(bf16x8 a, bf16x8 b, f32x4 c) { return __builtin_amdgcn_mfma_f32_16x16x32_bf16(a, b, c, 0, 0, 0); }
DEVI f32x16 mfma32(bf16x8 a, bf16x8 b, f32x16 c) { return __builtin_amdgcn_mfma_f32_32x32x16_bf16(a, b, c, 0, 0, 0); }
DEVI u32x4 lds16(lptr p) { return *(LAS u32x4*)p; }
DEVI void sts16(lptr p, u32x4 v) { *(LAS u32x4*)p = v; }
DEVI bf16x8 tr_frag(lptr p) {
    const s16x4 a = __builtin_amdgcn_ds_read_tr16_b64_v4i16((LAS s16x4*)p), b = __builtin_amdgcn_ds_read_tr16_b64_v4i16((LAS s16x4*)(p + 512));
    return __builtin_bit_cast(bf16x8, __builtin_shufflevector(a, b, 0, 1, 2, 3, 4, 5, 6, 7));
}
DEVI void swap16(unsigned& a, unsigned& b) { auto r = __builtin_amdgcn_permlane16_swap(a, b, false, false); a = r[0]; b = r[1]; }
DEVI void swap32(unsigned& a, unsigned& b) { auto r = __builtin_amdgcn_permlane32_swap(a, b, false, false); a = r[0]; b = r[1]; }
DEVI float sum_x16(float x) { unsigned a = __float_as_uint(x), b = a; swap16(a, b); return __uint_as_float(a) + __uint_as_float(b); }
DEVI float sum_x32(float x) { unsigned a = __float_as_uint(x), b = a; swap32(a, b); return __uint_as_float(a) + __uint_as_float(b); }
DEVI float max_x16(float x) { unsigned a = __float_as_uint(x), b = a; swap16(a, b); return fmaxf(__uint_as_float(a), __uint_as_float(b)); }
DEVI float max_x32(float x) { unsigned a = __float_as_uint(x), b = a; swap32(a, b); return fmaxf(__uint_as_float(a), __uint_as_float(b)); }
template <int C> DEVI float dppf(float x) { return __int_as_float(__builtin_amdgcn_mov_dpp(__float_as_int(x), C, 0xf, 0xf, true)); }
DEVI float nanmax(float a, float b) { return (a > b || a != a) ? a : b; }
DEVI float row_nanmax(float x) { x = nanmax(x, dppf<0x128>(x)); x = nanmax(x, dppf<0x124>(x)); x = nanmax(x, dppf<0x122>(x)); return nanmax(x, dppf<0x121>(x)); }
DEVI float row_sum(float x) { x += dppf<0x128>(x); x += dppf<0x124>(x); x += dppf<0x122>(x); return x + dppf<0x121>(x); }
DEVI void dma16(rsrc_t r, unsigned lds_addr, unsigned voff, unsigned soff) {
    asm volatile("s_mov_b32 m0, %0\n\ts_nop 0\n\tbuffer_load_dwordx4 %1, %2, %3 offen lds" :: "s"(lds_addr), "v"(voff), "s"(r), "s"(soff) : "m0", "memory"); }
DEVI unsigned short bld2(rsrc_t r, unsigned voff) { return __builtin_amdgcn_raw_buffer_load_b16(r, voff, 0, 0); }
DEVI void wg_barrier() { asm volatile("s_waitcnt lgkmcnt(0)\n\ts_barrier" ::: "memory"); }
DEVI void drain_vm() { asm volatile("s_waitcnt vmcnt(0)" ::: "memory"); }
DEVI unsigned long long mix64(unsigned long long z) {
    z += 0x9E3779B97F4A7C15ull; z = (z ^ (z >> 30)) * 0xBF58476D1CE4E5B9ull; z = (z ^ (z >> 27)) * 0x94D049BB133111EBull; return z ^ (z >> 31); }
DEVI void wait_tag(gu64* word, unsigned long long tag) {
    for (unsigned spins = 0; spins < SPIN_CAP; ++spins) {
        const unsigned long long v = __hip_atomic_load(word, RLX_AGENT);
        if (__all(v == tag)) break;
        __builtin_amdgcn_s_sleep(2);
    }
    asm volatile("" ::: "memory");
}
#define STAMP(i) do {} while (0)

DEVI f32x16 pv(bf16x8 v, f32x8 p, f32x16 o) {
    return __builtin_amdgcn_smfmac_f32_32x32x32_bf16(v, __builtin_bit_cast(bf16x16, p), o, 0xDDDD, 0, 0); }
DEVI f32x8 lo8(const f32x16& s) { return __builtin_shufflevector(s, s, 0, 1, 2, 3, 4, 5, 6, 7); }
DEVI f32x8 hi8(const f32x16& s) { return __builtin_shufflevector(s, s, 8, 9, 10, 11, 12, 13, 14, 15); }
DEVI void put_partial(lptr p, int lane, const f32x16& o, float m) {
    *(LAS f32x4*)(p + lane * 16) = (f32x4){o[0], o[1], o[2], o[3]};
    *(LAS f32x4*)(p + 1024 + lane * 16) = (f32x4){o[4], o[5], o[6], o[7]};
    *(LAS float*)(p + 2048 + lane * 4) = lane >= 32 ? m : o[8];
}
DEVI void attn_block_shifted(const bf16x8 (&kf)[8], const bf16x8 (&vf)[8][2], bf16x8 qf, f32x16& o, float& m_out) {
    f32x16 c = {};
    float m = -INFINITY;
#pragma unroll
    for (int t = 0; t < 8; ++t) { const f32x16 s = mfma32(kf[t], qf, c);
#pragma unroll
        for (int r = 0; r < 16; ++r) m = fmaxf(m, s[r]); }
    m = max_x32(m);
#pragma unroll
    for (int r = 0; r < 16; ++r) c[r] = -m;
    o = f32x16{};
#pragma unroll
    for (int t = 0; t < 8; ++t) {
        f32x16 s = mfma32(kf[t], qf, c);
#pragma unroll
        for (int r = 0; r < 16; ++r) s[r] = __builtin_amdgcn_exp2f(s[r]);
        o = pv(vf[t][0], lo8(s), o); o = pv(vf[t][1], hi8(s), o);
    }
    m_out = m;
}
typedef unsigned u32x16 __attribute__((ext_vector_type(16)));
#define R(b, i) "v[" #b "+" #i "]"
#define RR(b, i, n) "v[" #b "+" #i ":" #b "+" #i "+" #n "]"
#define A_EX(c, i) "v_exp_f32 " R(c, i) ", " R(c, i) "\n\t"
#define A_PV(o, vi, p, pi) "v_smfmac_f32_32x32x32_bf16 " RR(o, 0, 15) ", " RR(192, vi, 3) ", " RR(p, pi, 7) ", v83\n\t"
#define A_QK(d, ki, q) "v_mfma_f32_32x32x16_bf16 " RR(d, 0, 15) ", " RR(160, ki, 3) ", " RR(q, 0, 3) ", 0\n\t"
#define A_ZERO(o) "v_mfma_f32_32x32x16_bf16 " RR(o, 0, 15) ", v[84:87], v[84:87], 0\n\t"
#define A_TILE(c, p, oc, op, vprev, vcur, knext, q, X0, X1) \
    A_EX(c, 0) A_EX(c, 1) A_PV(op, vprev, p, 8) X0 A_EX(c, 2) A_EX(c, 3) A_EX(c, 4) A_QK(p, knext, q) \
    A_EX(c, 5) A_EX(c, 6) A_EX(c, 7) A_EX(c, 8) A_EX(c, 9) A_EX(c, 10) A_PV(oc, vcur, c, 0) X1 A_EX(c, 11) A_EX(c, 12) A_EX(c, 13) A_EX(c, 14) A_EX(c, 15)
#define A_PUT(o, a16, a4) "ds_write_b128 " a16 ", " RR(o, 0, 3) "\n\tds_write_b128 " a16 ", " RR(o, 4, 3) " offset:1024\n\tds_write_b32 " a4 ", " R(o, 8) " offset:2048\n\t"
#define A_BLOCK(o, op, qc, qn, qoff, a16, a4) \
    A_TILE(128, 144, o, op, 60, 0, 4, qc, A_ZERO(o), "ds_read_b128 " RR(qn, 0, 3) ", v82 offset:" #qoff "\n\t") \
    A_TILE(144, 128, o, o, 4, 8, 8, qc, "", A_PUT(op, a16, a4)) \
    A_TILE(128, 144, o, o, 12, 16, 12, qc, "", "") A_TILE(144, 128, o, o, 20, 24, 16, qc, "", "") \
    A_TILE(128, 144, o, o, 28, 32, 20, qc, "", "") A_TILE(144, 128, o, o, 36, 40, 24, qc, "", "") \
    A_TILE(128, 144, o, o, 44, 48, 28, qc, "s_waitcnt lgkmcnt(0)\n\t", "") A_TILE(144, 128, o, o, 52, 56, 0, qn, "", "")
#define CL8(a, b, c, d, e, f, g, h) "v" #a, "v" #b, "v" #c, "v" #d, "v" #e, "v" #f, "v" #g, "v" #h
DEVI void attn_fast(u32x16 k0, u32x16 k1, u32x16 v0, u32x16 v1, u32x16 v2, u32x16 v3, unsigned qaddr, unsigned p16, unsigned p4) {
    unsigned cnt, pa16 = p16 + 7 * 8 * P_SZ, pa4 = p4 + 7 * 8 * P_SZ;
    asm volatile(
        "ds_read_b128 v[88:91], v82\n\t"
        "v_mov_b32 v83, 0xdddd\n\tv_mov_b32 v84, 0\n\tv_mov_b32 v85, 0\n\tv_mov_b32 v86, 0\n\tv_mov_b32 v87, 0\n\t"
        "s_mov_b32 %0, 4\n\t"
        "s_waitcnt lgkmcnt(0)\n\t"
        A_QK(128, 0, 88)
        "s_nop 11\n"
        "1:\n\t"
        A_BLOCK(96, 112, 88, 92, 1024, "v81", "v79")
        A_BLOCK(112, 96, 92, 88, 2048, "v80", "v78")
        "v_add_u32 v82, 0x800, v82\n\t"
        "v_add_u32 v81, 0x4800, v80\n\tv_add_u32 v79, 0x4800, v78\n\t"
        "v_add_u32 v80, 0x9000, v80\n\tv_add_u32 v78, 0x9000, v78\n\t"
        "s_sub_u32 %0, %0, 1\n\ts_cmp_lg_u32 %0, 0\n\ts_cbranch_scc1 1b\n\t"
        A_PV(112, 60, 144, 8)
        "s_nop 15\n\t"
        A_PUT(112, "v81", "v79")
        "s_waitcnt lgkmcnt(0)"
        : "=&s"(cnt), "+{v82}"(qaddr), "+{v81}"(pa16), "+{v80}"(p16), "+{v79}"(pa4), "+{v78}"(p4)
        : "{v[160:175]}"(k0), "{v[176:191]}"(k1), "{v[192:207]}"(v0), "{v[208:223]}"(v1), "{v[224:239]}"(v2), "{v[240:255]}"(v3)
        : "memory", "scc", CL8(83, 84, 85, 86, 87, 88, 89, 90), CL8(91, 92, 93, 94, 95, 96, 97, 98), CL8(99, 100, 101, 102, 103, 104, 105, 106),
          CL8(107, 108, 109, 110, 111, 112, 113, 114), CL8(115, 116, 117, 118, 119, 120, 121, 122), CL8(123, 124, 125, 126, 127, 128, 129, 130),
          CL8(131, 132, 133, 134, 135, 136, 137, 138), CL8(139, 140, 141, 142, 143, 144, 145, 146), CL8(147, 148, 149, 150, 151, 152, 153, 154),
          "v155", "v156", "v157", "v158", "v159");
}
static_assert(8 * P_SZ == 0x4800, "partial stride");
DEVI u32x16 cat4(u32x4 a, u32x4 b, u32x4 c, u32x4 d) {
    return (u32x16){a[0], a[1], a[2], a[3], b[0], b[1], b[2], b[3], c[0], c[1], c[2], c[3], d[0], d[1], d[2], d[3]}; }
DEVI u32x4 asu(bf16x8 v) { return __builtin_bit_cast(u32x4, v); }

__global__ __launch_bounds__(512, 2) void fused_kernel(
    const unsigned short* __restrict__ x, const unsigned short* __restrict__ wqkv, const unsigned short* __restrict__ wo,
    const unsigned short* __restrict__ u, const unsigned short* __restrict__ lar, const unsigned short* __restrict__ aim,
    const unsigned short* __restrict__ cre, const unsigned short* __restrict__ cim, const unsigned short* __restrict__ ldt,
    const unsigned short* __restrict__ dsk, unsigned short* __restrict__ out, char* __restrict__ ws_) {
    extern __shared__ __attribute__((aligned(16))) char smem_[];
    const lptr lds = (lptr)smem_;
    GAS char* ws = (GAS char*)ws_;
    const int tid = threadIdx.x, lane = tid & 63, w = __builtin_amdgcn_readfirstlane(tid >> 6);
    const int r16 = lane & 15, g = lane >> 4, l31 = lane & 31, hh = lane >> 5;
    const int xcd = blockIdx.x & 7, sl = blockIdx.x >> 3, h = 4 * xcd + (sl >> 3), j = sl & 7, d0 = 64 * xcd + 2 * sl;
    const rsrc_t rws = mk_rsrc(ws_, WS_USED);
    STAMP(0);
    unsigned long long nonce = __hip_atomic_load((gu64*)(ws + WS_NONCE), RLX_AGENT);
    unsigned long long tag;

    const int kch = (w >> 1) & 1, kn = tid & 127;
    unsigned short p_lar, p_aim, p_cre, p_cim, p_ldt;
    { const unsigned o = ((d0 + kch) * 128 + kn) * 2;
      p_lar = bld2(mk_rsrc(lar, 131072), o); p_aim = bld2(mk_rsrc(aim, 131072), o); p_cre = bld2(mk_rsrc(cre, 131072), o); p_cim = bld2(mk_rsrc(cim, 131072), o);
      p_ldt = bld2(mk_rsrc(ldt, 1024), (d0 + kch) * 2); }

    {
        const int hq = 4 * (xcd & 1) + (sl >> 3), rb = 8 * (xcd >> 1) + (sl & 7), koff = (sl & 7) + 2 * (sl >> 3);
        const rsrc_t rw = mk_rsrc(wqkv, 512 * 1536 * 2), rx = mk_rsrc(x, 2048 * 512 * 2);
        const unsigned lds0 = (unsigned)(uintptr_t)lds;
        const int rl = lane >> 3, c8 = lane & 7;
        const unsigned xv = rl * 1024 + ((c8 ^ rl) * 16);
        const unsigned wv = rl * 3072 + (((c8 >> 1) ^ (((rl >> 1) & 1) + 2 * (w & 1))) * 32) + (c8 & 1) * 16;
        auto issue = [&](int i) {
            const int kc = (i + koff) & 7; const unsigned dst = lds0 + L_RING + (i & 3) * SLOT + w * 1024;
            dma16(rx, dst, xv, (64 * rb + 8 * w) * 1024 + kc * 128);
            const unsigned ws0 = (64 * kc + 8 * w) * 3072 + hq * 128;
            dma16(rw, dst + 8192, wv, ws0); dma16(rw, dst + 16384, wv, ws0 + 1024); dma16(rw, dst + 24576, wv, ws0 + 2048); };
        issue(0); issue(1); issue(2);
        if (tid < 9) *(LAS unsigned*)(lds + L_MISC + (tid == 8 ? 32 : 64 + 4 * tid)) = 0u;
        const int tg = w & 1, hl = w >> 1, hp = 4 * hq + hl;
        const int xa = (32 * tg + r16) * 128 + ((g ^ (r16 & 7)) * 16);
        const int wa = 8192 + (8 * g + (r16 >> 2)) * 128 + ((hl ^ (((r16 >> 3) & 1) + 2 * (g & 1))) * 32) + (r16 & 3) * 8;
        f32x4 aq[2] = {}, ak[2] = {}, av[2] = {};
#pragma unroll 1
        for (int i = 0; i < 8; ++i) {
            if (i < 6) asm volatile("s_waitcnt vmcnt(8)" ::: "memory"); else if (i == 6) asm volatile("s_waitcnt vmcnt(4)" ::: "memory"); else drain_vm();
            wg_barrier();
            if (i == 0) STAMP(1);
            if (i < 5) issue(i + 3);
            const lptr sp = lds + L_RING + (i & 3) * SLOT;
#pragma unroll
            for (int ks = 0; ks < 2; ++ks) {
                const lptr wp = sp + wa + ks * 4096, xp = sp + (xa ^ (ks * 64));
                const bf16x8 fq = tr_frag(wp), fk = tr_frag(wp + 8192), fv = tr_frag(wp + 16384);
#pragma unroll
                for (int tb = 0; tb < 2; ++tb) { const bf16x8 xb = asb(lds16(xp + tb * 2048));
                    aq[tb] = mfma16(fq, xb, aq[tb]); ak[tb] = mfma16(fk, xb, ak[tb]); av[tb] = mfma16(xb, fv, av[tb]); }
            }
        }
        STAMP(2);
        asm volatile("" : "+v"(nonce));
        tag = mix64(nonce);
        float q2 = 0.f, k2 = 0.f;
        u32x4 qw, kw, vw;
#pragma unroll
        for (int tb = 0; tb < 2; ++tb) {
            const f32x4 q = aq[tb] * QSCALE, k = ak[tb], v = av[tb];
            q2 = nanmax(q2, sum_x32(sum_x16(q[0] * q[0] + q[1] * q[1] + q[2] * q[2] + q[3] * q[3])));
            k2 = nanmax(k2, sum_x32(sum_x16(k[0] * k[0] + k[1] * k[1] + k[2] * k[2] + k[3] * k[3])));
            qw[2 * tb] = pk2(q[0], q[1]); qw[2 * tb + 1] = pk2(q[2], q[3]);
            kw[2 * tb] = pk2(k[0], k[1]); kw[2 * tb + 1] = pk2(k[2], k[3]);
            vw[2 * tb] = pk2(v[0], v[1]); vw[2 * tb + 1] = pk2(v[2], v[3]);
        }
        auto sw = [](u32x4 a) { unsigned a0 = a[0], a1 = a[1], b0 = a[2], b1 = a[3]; swap16(a0, b0); swap16(a1, b1); return (u32x4){a0, a1, b0, b1}; };
        qw = sw(qw); kw = sw(kw); vw = sw(vw);
        q2 = row_nanmax(q2); k2 = row_nanmax(k2);
        const int tile = (hp * 8 + (rb >> 2)) * 8 + 2 * (rb & 3) + tg;
        const unsigned kvb = WS_QKV + tile * 3072;
        const unsigned kpos = (g >> 1) * 512 + (16 * (g & 1) + r16) * 16;
        bst16_sc1(qw, rws, kpos, kvb);
        bst16_sc1(kw, rws, kpos, kvb + 1024);
        bst16_sc1(vw, rws, (g & 1) * 512 + (g >> 1) * 256 + r16 * 16, kvb + 2048);
        if (lane < 2) __hip_atomic_store((GAS unsigned*)(ws + WS_NRM) + tile * 2 + lane, __float_as_uint(lane ? k2 : q2), RLX_AGENT);
        drain_vm();
        wg_barrier();
        if (tid == 0) __hip_atomic_store((gu64*)(ws + WS_FLAG_P + (hq * 32 + rb) * 128), tag, RLX_AGENT);
        STAMP(3);
    }

    {
        const int role = w >> 2;
        const float dt = rbf(expf(bf2f(p_ldt)));
        const float zr = -rbf(expf(bf2f(p_lar))) * dt, zi = bf2f(p_aim) * dt;
        const f32x2 c = {2.f * bf2f(p_cre) * dt, 2.f * bf2f(p_cim) * dt};
        const float zl = zr * 1.4426950408889634f, zt = zi * 0.15915494309189535f;
        auto cexp = [&](float p) { const float mag = __builtin_amdgcn_exp2f(zl * p), a = __builtin_amdgcn_fractf(zt * p);
            return (f32x2){mag * __builtin_amdgcn_cosf(a), mag * __builtin_amdgcn_sinf(a)}; };
        auto cmul = [](f32x2 a, f32x2 fr, f32x2 fi) { return a * fr + (f32x2){a[1], a[0]} * fi; };
        const f32x2 e = cexp(32.f), er = {e[0], e[0]}, ei = {-e[1], e[1]};
        const lptr xp = lds + L_XY + kch * XY_CH + kn * 4;
        if (role == 0) {
            f32x2 a = c;
#pragma unroll
            for (int i = 0; i < 48; ++i) { *(LAS unsigned*)(xp + i * XY_ROW) = pk2(a[0], a[1]); a = cmul(a, er, ei); }
        } else {
            const f32x2 s = cexp(1536.f), f = cexp(1.f), fr = {f[0], f[0]}, fi = {f[1], -f[1]};
            f32x2 a = cmul(c, (f32x2){s[0], s[0]}, (f32x2){-s[1], s[1]}), y = {1.f, 0.f};
#pragma unroll
            for (int i = 0; i < 32; ++i) {
                if (i < 16) { *(LAS unsigned*)(xp + (48 + i) * XY_ROW) = pk2(a[0], a[1]); a = cmul(a, er, ei); }
                *(LAS unsigned*)(xp + (64 + i) * XY_ROW) = pk2(y[0], y[1]); y = cmul(y, fr, fi);
            }
            const float sm = sum_x32(sum_x16(row_sum(a[0])));
            if (lane == 0) *(LAS float*)(lds + L_MISC + 48 + 4 * (w & 3)) = sm;
        }
        wg_barrier();
        STAMP(4);
    }

    bf16x8 kf[8], vf[8][2];
    float k2w;
    {
        wait_tag((gu64*)(ws + WS_FLAG_P + ((h >> 2) * 32 + (lane == 4 ? 4 * j + (w >> 1) : 4 * w + (lane & 3))) * 128), tag);
        STAMP(5);
        const unsigned kvb = WS_QKV + (h * 8 + w) * 8 * 3072;
        const unsigned vpos = ((lane >> 4) & 1) * 512 + hh * 256 + r16 * 16;
        const u32x4 qv = bld16_sc1(rws, lane * 16, WS_QKV + ((h * 8 + j) * 8 + w) * 3072);
        const unsigned q2v = __builtin_amdgcn_raw_buffer_load_b32(rws, 0, WS_NRM + ((h * 8 + j) * 8 + w) * 8, AUX_SC1);
        k2w = __uint_as_float(__builtin_amdgcn_raw_buffer_load_b32(rws, (lane & 7) * 8 + 4, WS_NRM + (h * 8 + w) * 64, AUX_SC1));
        u32x4 vr[8];
#pragma unroll
        for (int t = 0; t < 8; ++t) { kf[t] = asb(bld16_sc1(rws, lane * 16 + 1024, kvb + t * 3072)); vr[t] = bld16_sc1(rws, vpos + 2048, kvb + t * 3072); }

        {
            const int ch = w >> 2, mb = w & 3;
            const lptr ap = lds + L_XY + ch * XY_CH + (16 * mb + r16) * XY_ROW + g * 16;
            const lptr bp = lds + L_XY + ch * XY_CH + (64 + r16) * XY_ROW + g * 16;
            f32x4 a0 = {}, a1 = {};
#pragma unroll
            for (int ks = 0; ks < 8; ++ks) { const bf16x8 a = asb(lds16(ap + ks * 64));
                a0 = mfma16(a, asb(lds16(bp + ks * 64)), a0); a1 = mfma16(a, asb(lds16(bp + 16 * XY_ROW + ks * 64)), a1); }
            const lptr kp = lds + L_KD + ch * KD_CH + ((16 * mb + 4 * g) * 32 + r16) * 2;
#pragma unroll
            for (int i = 0; i < 4; ++i) { const unsigned v = pk2(a0[i], a1[i]);
                *(LAS unsigned short*)(kp + i * 64) = (unsigned short)v; *(LAS unsigned short*)(kp + i * 64 + 32) = (unsigned short)(v >> 16); }
            if (lane == 0 && mb == 0) { const float s = *(LAS float*)(lds + L_MISC + 48 + 8 * ch) + *(LAS float*)(lds + L_MISC + 52 + 8 * ch);
                *(LAS unsigned short*)(lds + L_KD + ch * KD_CH + 4096) = (unsigned short)pk2(s, 0.f); }
        }
        sts16(lds + L_Q + w * 1024 + lane * 16, qv);
        if (lane == 0) *(LAS unsigned*)(lds + L_MISC + 4 * w) = q2v;
        wg_barrier();
#pragma unroll
        for (int t = 0; t < 8; ++t) {
            u32x4 one = {0x3F803F80u, 0x3F803F80u, 0x3F803F80u, 0x3F803F80u};
            unsigned a[4], b[4];
#pragma unroll
            for (int i = 0; i < 4; ++i) { a[i] = vr[t][i]; b[i] = one[i]; swap16(a[i], b[i]); }
            vf[t][0] = asb((u32x4){a[0], a[1], a[2], a[3]}); vf[t][1] = asb((u32x4){b[0], b[1], b[2], b[3]});
        }
    }
    unsigned uu[4];
    unsigned kr[8];
    {
        const int ch = tid >> 8, y = tid & 255;
        const lptr kp = lds + L_KD + ch * KD_CH;
#pragma unroll
        for (int i = 0; i < 8; ++i) {
            const int l0 = 2047 - 8 * y - 2 * i, l1 = l0 - 1;
            const unsigned pp = pk2((float)l0, (float)l1);
            const int p0 = (int)bf_lo(pp), p1 = (int)bf_hi(pp);
            const unsigned v0 = l0 >= 0 ? *(LAS unsigned short*)(kp + 2 * max(p0, 0)) : 0u, v1 = l1 >= 0 ? *(LAS unsigned short*)(kp + 2 * max(p1, 0)) : 0u;
            kr[i] = v0 | (v1 << 16);
        }
        wg_barrier();
        STAMP(6);
    }

    {
        float q2 = *(LAS float*)(lds + L_MISC + 4 * (lane & 7));
        q2 = nanmax(q2, dppf<0x124>(q2)); q2 = nanmax(q2, dppf<0x122>(q2)); q2 = nanmax(q2, dppf<0x121>(q2));
        k2w = nanmax(k2w, dppf<0x124>(k2w)); k2w = nanmax(k2w, dppf<0x122>(k2w)); k2w = nanmax(k2w, dppf<0x121>(k2w));
        const bool fast = __builtin_amdgcn_readfirstlane((q2 * k2w * 1.05f <= SAFE2) ? 1 : 0) != 0;
        const lptr qp = lds + L_Q + lane * 16;
        const lptr pp = lds + L_P + w * P_SZ;
#pragma unroll
        for (int i = 0; i < 4; ++i) uu[i] = __builtin_amdgcn_raw_buffer_load_b32(mk_rsrc(u, 2048 * 1024), (4 * tid + i) * 1024, d0 * 2, 0);
        if (__builtin_expect(fast, 1)) {
            const unsigned l0 = (unsigned)(uintptr_t)lds;
            attn_fast(cat4(asu(kf[0]), asu(kf[1]), asu(kf[2]), asu(kf[3])), cat4(asu(kf[4]), asu(kf[5]), asu(kf[6]), asu(kf[7])),
                      cat4(asu(vf[0][0]), asu(vf[0][1]), asu(vf[1][0]), asu(vf[1][1])), cat4(asu(vf[2][0]), asu(vf[2][1]), asu(vf[3][0]), asu(vf[3][1])),
                      cat4(asu(vf[4][0]), asu(vf[4][1]), asu(vf[5][0]), asu(vf[5][1])), cat4(asu(vf[6][0]), asu(vf[6][1]), asu(vf[7][0]), asu(vf[7][1])),
                      l0 + L_Q + lane * 16, l0 + L_P + w * P_SZ + lane * 16, l0 + L_P + w * P_SZ + lane * 4);
        } else {
            if (lane == 0) { *(LAS unsigned*)(lds + L_MISC + 32) = 1u; *(LAS unsigned*)(lds + L_MISC + 64 + 4 * w) = 1u; }
#pragma unroll 1
            for (int qb = 0; qb < 8; ++qb) {
                f32x16 o; float m;
                attn_block_shifted(kf, vf, asb(lds16(qp + qb * 1024)), o, m);
                put_partial(pp + qb * (8 * P_SZ), lane, o, m);
            }
        }
        STAMP(7);
        wg_barrier();
        STAMP(8);
        const lptr p = lds + L_P + w * (8 * P_SZ);
        f32x4 oa = {}, ob = {}; float l = 0.f;
        if (*(LAS unsigned*)(lds + L_MISC + 32) == 0u) {
#pragma unroll
            for (int i = 0; i < 8; ++i) { oa += *(LAS f32x4*)(p + i * P_SZ + lane * 16); ob += *(LAS f32x4*)(p + i * P_SZ + 1024 + lane * 16);
                l += *(LAS float*)(p + i * P_SZ + 2048 + l31 * 4); }
        } else {
            float mi[8], mx = -INFINITY;
#pragma unroll
            for (int i = 0; i < 8; ++i) { mi[i] = *(LAS unsigned*)(lds + L_MISC + 64 + 4 * i) ? *(LAS float*)(p + i * P_SZ + 2048 + 128 + l31 * 4) : 0.f; mx = fmaxf(mx, mi[i]); }
#pragma unroll
            for (int i = 0; i < 8; ++i) { const float f = __builtin_amdgcn_exp2f(mi[i] - mx);
                oa += *(LAS f32x4*)(p + i * P_SZ + lane * 16) * f; ob += *(LAS f32x4*)(p + i * P_SZ + 1024 + lane * 16) * f;
                l += *(LAS float*)(p + i * P_SZ + 2048 + l31 * 4) * f; }
        }
        const float rl = 1.f / l;
        oa *= rl; ob *= rl;
        unsigned a0 = pk2(oa[0], oa[1]), a1 = pk2(oa[2], oa[3]), b0 = pk2(ob[0], ob[1]), b1 = pk2(ob[2], ob[3]);
        swap32(a0, b0); swap32(a1, b1);
        const u32x4 ow = {a0, a1, b0, b1};
        bst16_sc1(ow, rws, (l31 >> 4) * 16384 + hh * 256 + (l31 & 15) * 16, WS_O + (16 * j + 2 * w) * 16384 + h * 512);
        wg_barrier();
    }

    {
        const int ch = tid >> 8, y = tid & 255;
        unsigned sft[7];
#pragma unroll
        for (int i = 0; i < 7; ++i) sft[i] = __builtin_amdgcn_alignbit(kr[i + 1], kr[i], 16);
        const lptr kp = lds + L_KR + ch * KR_CH + y * 16;
#pragma unroll
        for (int c = 0; c < 4; ++c) {
            sts16(kp + (2 * c) * KR_CP, (u32x4){kr[c], kr[c + 1], kr[c + 2], kr[c + 3]});
            sts16(kp + (2 * c + 1) * KR_CP, (u32x4){sft[c], sft[c + 1], sft[c + 2], sft[c + 3]});
        }
        if (tid < 16) sts16(lds + L_KR + (tid >> 3) * KR_CH + (tid & 7) * KR_CP + 4096, (u32x4){0u, 0u, 0u, 0u});
        if (tid < 64) sts16(lds + L_U + (tid >> 5) * U_CH + (tid & 31) * 16, (u32x4){0u, 0u, 0u, 0u});
        const unsigned sel_lo = 0x05040100u, sel_hi = 0x07060302u;
        *(LAS u32x2*)(lds + L_U + 512 + tid * 8) = (u32x2){__builtin_amdgcn_perm(uu[1], uu[0], sel_lo), __builtin_amdgcn_perm(uu[3], uu[2], sel_lo)};
        *(LAS u32x2*)(lds + L_U + U_CH + 512 + tid * 8) = (u32x2){__builtin_amdgcn_perm(uu[1], uu[0], sel_hi), __builtin_amdgcn_perm(uu[3], uu[2], sel_hi)};
        drain_vm();
        wg_barrier();
        if (tid == 0) __hip_atomic_store((gu64*)(ws + WS_FLAG_O + (j * 32 + h) * 8), tag, RLX_AGENT);
        STAMP(9);
    }

    {
        const int ncol0 = 256 * (xcd >> 2) + 64 * (h & 3);
        const rsrc_t rwo = mk_rsrc(wo, 512 * 512 * 2);
        u32x4 wr[8];
        const unsigned wv = ((8 * ((lane >> 3) & 3) + 4 * hh + ((lane >> 1) & 3)) * 512 + 8 * (lane & 1)) * 2;
#pragma unroll
        for (int i = 0; i < 8; ++i) wr[i] = bld16(rwo, wv + (i & 3) * 32, (32 * (2 * w + (i >> 2)) * 512 + ncol0) * 2);

        const int ch = w >> 2, cs = (w >> 1) & 1, rg = w & 1;
        const lptr ab = lds + L_KR + ch * KR_CH + (7 - (r16 & 7)) * KR_CP + 2 * (2040 - 8 * (r16 >> 3) - 16 * (g >> 1) + 8 * (g & 1));
        const lptr bb = lds + L_U + ch * U_CH + 512 + 32 * (r16 - (g >> 1)) + 16 * (g & 1);
        f32x4 acc[4] = {};
        auto conv = [&](auto CS) {
            constexpr int cs_ = decltype(CS)::value;
            constexpr int C0 = cs_ ? 1 : 0, C1 = cs_ ? 2 : 3, C2 = cs_ ? 5 : 4, C3 = cs_ ? 6 : 7;
#pragma unroll 1
            for (int rr = 0; rr < 4; ++rr) {
                const int r = 4 * rg + rr;
                const lptr ap = ab - 64 * r - 3584, bp = bb - 64 * r;
                bf16x8 A[8], B[8];
#pragma unroll
                for (int m = 0; m <= C3; ++m) { A[m] = asb(lds16(ap + 512 * (7 - m))); B[m] = asb(lds16(bp + 512 * m)); }
#pragma unroll
                for (int m = 0; m <= C3; ++m) {
                    if (m <= C0) acc[0] = mfma16(A[m], B[C0 - m < 0 ? 0 : C0 - m], acc[0]);
                    if (m <= C1) acc[1] = mfma16(A[m], B[C1 - m < 0 ? 0 : C1 - m], acc[1]);
                    if (m <= C2) acc[2] = mfma16(A[m], B[C2 - m < 0 ? 0 : C2 - m], acc[2]);
                    acc[3] = mfma16(A[m], B[C3 - m], acc[3]);
                }
            }
        };
        if (cs) conv(std::integral_constant<int, 1>{}); else conv(std::integral_constant<int, 0>{});
#pragma unroll
        for (int i = 0; i < 8; ++i) sts16(lds + L_WO + ((2 * w + (i >> 2)) * 4 + (i & 3)) * 1024 + lane * 16, wr[i]);
        STAMP(10);
        wg_barrier();
        {
            const lptr cp = lds + L_CP + (ch * 2 + rg) * 8192 + (4 * r16 + g) * 16;
            const int c0 = cs ? 1 : 0, c1 = cs ? 2 : 3, c2 = cs ? 5 : 4, c3 = cs ? 6 : 7;
            *(LAS f32x4*)(cp + c0 * 1024) = acc[0]; *(LAS f32x4*)(cp + c1 * 1024) = acc[1];
            *(LAS f32x4*)(cp + c2 * 1024) = acc[2]; *(LAS f32x4*)(cp + c3 * 1024) = acc[3];
        }
        if (w == 0) wait_tag((gu64*)(ws + WS_FLAG_O + (j * 32 + l31) * 8), tag);
        wg_barrier();
        STAMP(11);
        if (blockIdx.x == 0 && tid == 0) __hip_atomic_store((gu64*)(ws + WS_NONCE), tag, RLX_AGENT);
        const int rgp = w & 3, c2h = w >> 2, row0 = 256 * j + 64 * (xcd & 3) + 16 * rgp;
        u32x4 of[16];
        const unsigned ov = lane * 16;
#pragma unroll
        for (int ks = 0; ks < 16; ++ks) of[ks] = bld16_sc1(rws, ov + ks * 1024, WS_O + (row0 >> 4) * 16384);
        {
            const lptr cp = lds + L_CP + tid * 16;
            const f32x4 y0 = *(LAS f32x4*)cp + *(LAS f32x4*)(cp + 8192), y1 = *(LAS f32x4*)(cp + 16384) + *(LAS f32x4*)(cp + 24576);
            const unsigned dd = *(const unsigned*)(dsk + d0);
            const float s0 = bf_lo(dd), s1 = bf_hi(dd);
#pragma unroll
            for (int i = 0; i < 4; ++i) {
                const unsigned yy = pk2(y0[i], y1[i]), us = pk2(bf_lo(uu[i]) * s0, bf_hi(uu[i]) * s1);
                *(unsigned*)(out + (4 * tid + i) * 1024 + 512 + d0) = pk2(bf_lo(yy) + bf_lo(us), bf_hi(yy) + bf_hi(us));
            }
        }
        f32x4 p0 = {}, p1 = {};
        const lptr wp = lds + L_WO + (2 * c2h) * 1024 + lane * 8;
#pragma unroll
        for (int ks = 0; ks < 16; ++ks) { const bf16x8 b = asb(of[ks]);
            p0 = mfma16(tr_frag(wp + ks * 4096), b, p0); p1 = mfma16(tr_frag(wp + ks * 4096 + 1024), b, p1); }
        unsigned a0 = pk2(p0[0], p0[1]), a1 = pk2(p0[2], p0[3]), b0 = pk2(p1[0], p1[1]), b1 = pk2(p1[2], p1[3]);
        swap16(a0, b0); swap16(a1, b1);
        *(u32x4*)(out + (row0 + r16) * 1024 + ncol0 + 32 * c2h + 16 * (g & 1) + 8 * (g >> 1)) = (u32x4){a0, a1, b0, b1};
        STAMP(12);
    }
}

extern "C" void kernel_launch(void* const* d_in, const int* in_sizes, int n_in, void* d_out, int out_size, void* d_ws, size_t ws_size, hipStream_t stream) {
    (void)in_sizes; (void)n_in; (void)out_size; (void)ws_size;
    (void)hipFuncSetAttribute((const void*)fused_kernel, hipFuncAttributeMaxDynamicSharedMemorySize, LDS_BYTES);
    typedef const unsigned short* cp;
    fused_kernel<<<256, 512, LDS_BYTES, stream>>>((cp)d_in[0], (cp)d_in[1], (cp)d_in[2], (cp)d_in[3], (cp)d_in[4], (cp)d_in[5], (cp)d_in[6], (cp)d_in[7],
                                                  (cp)d_in[8], (cp)d_in[9], (unsigned short*)d_out, (char*)d_ws);
}
```

```cpp
#include <hip/hip_runtime.h>
#include <hip/hip_bf16.h>
#include <stdint.h>
#define NPASS 1

#define GAS __attribute__((address_space(1)))
#define LAS __attribute__((address_space(3)))
#define DEVI __device__ __forceinline__
typedef __bf16 bf16x2 __attribute__((ext_vector_type(2)));
typedef __bf16 bf16x8 __attribute__((ext_vector_type(8)));
typedef __bf16 bf16x16 __attribute__((ext_vector_type(16)));
typedef float f32x2 __attribute__((ext_vector_type(2)));
typedef float f32x4 __attribute__((ext_vector_type(4)));
typedef float f32x8 __attribute__((ext_vector_type(8)));
typedef float f32x16 __attribute__((ext_vector_type(16)));
typedef unsigned u32x2 __attribute__((ext_vector_type(2)));
typedef unsigned u32x4 __attribute__((ext_vector_type(4)));
typedef short s16x4 __attribute__((ext_vector_type(4)));
typedef short s16x8 __attribute__((ext_vector_type(8)));
typedef GAS unsigned long long gu64;
typedef LAS char* lptr;
typedef __amdgpu_buffer_rsrc_t rsrc_t;

constexpr unsigned WS_NONCE = 0;
constexpr unsigned WS_FLAG_P = 4096;
constexpr unsigned WS_FLAG_O = WS_FLAG_P + 256 * 128;
constexpr unsigned WS_NRM = WS_FLAG_O + 2048;
constexpr unsigned WS_QKV = 65536;
constexpr unsigned WS_O = WS_QKV + 32 * 64 * 3072;
constexpr unsigned WS_USED = WS_O + 2048 * 512 * 2;
constexpr unsigned WS_DBG = 16u << 20;

constexpr int L_RING = 0, SLOT = 32768;
constexpr int L_XY = 0, XY_ROW = 528, XY_CH = 96 * XY_ROW;
constexpr int L_KD = 2 * XY_CH, KD_CH = 4112;
constexpr int L_P = 0, P_SZ = 2304;
constexpr int L_Q = 147456;
constexpr int L_MISC = 155648;
constexpr int L_KR = 0, KR_CP = 4160, KR_CH = 8 * KR_CP;
constexpr int L_U = 2 * KR_CH, U_CH = 4608;
constexpr int L_WO = L_U + 2 * U_CH;
constexpr int L_CP = 0;
constexpr int LDS_BYTES = 163840;
static_assert(L_KD + 2 * KD_CH <= L_Q && L_WO + 65536 <= L_Q && 64 * P_SZ <= L_Q, "LDS map");

constexpr float QSCALE = 0.25f * 1.4426950408889634f;
constexpr float SAFE2 = 60.f * 60.f;
constexpr unsigned SPIN_CAP = 1u << 18;
constexpr int AUX_SC1 = 16;

#define SBAR() __builtin_amdgcn_sched_barrier(0)
#define RLX_AGENT __ATOMIC_RELAXED, __HIP_MEMORY_SCOPE_AGENT

DEVI rsrc_t mk_rsrc(const void* p, unsigned bytes) { return __builtin_amdgcn_make_buffer_rsrc((void*)p, (short)0, (int)bytes, 0x00020000); }
DEVI u32x4 bld16(rsrc_t r, unsigned voff, unsigned soff) { return __builtin_amdgcn_raw_buffer_load_b128(r, voff, soff, 0); }
DEVI u32x4 bld16_sc1(rsrc_t r, unsigned voff, unsigned soff) { return __builtin_amdgcn_raw_buffer_load_b128(r, voff, soff, AUX_SC1); }
DEVI void bst16_sc1(u32x4 v, rsrc_t r, unsigned voff, unsigned soff) { __builtin_amdgcn_raw_buffer_store_b128(v, r, voff, soff, AUX_SC1); }
DEVI unsigned pk2(float lo, float hi) { f32x2 v = {lo, hi}; return __builtin_bit_cast(unsigned, __builtin_convertvector(v, bf16x2)); }
DEVI float bf_lo(unsigned v) { return __uint_as_float(v << 16); }
DEVI float bf_hi(unsigned v) { return __uint_as_float(v & 0xffff0000u); }
DEVI float bf2f(unsigned short v) { return __uint_as_float((unsigned)v << 16); }
DEVI float rbf(float x) { return bf_lo(pk2(x, 0.f)); }
DEVI bf16x8 asb(u32x4 v) { return __builtin_bit_cast(bf16x8, v); }
DEVI f32x4 mfma16(bf16x8 a, bf16x8 b, f32x4 c) { return __builtin_amdgcn_mfma_f32_16x16x32_bf16(a, b, c, 0, 0, 0); }
DEVI f32x16 mfma32(bf16x8 a, bf16x8 b, f32x16 c) { return __builtin_amdgcn_mfma_f32_32x32x16_bf16(a, b, c, 0, 0, 0); }
DEVI u32x4 lds16(lptr p) { return *(LAS u32x4*)p; }
DEVI void sts16(lptr p, u32x4 v) { *(LAS u32x4*)p = v; }
DEVI bf16x8 tr_frag(lptr p) {
    const s16x4 a = __builtin_amdgcn_ds_read_tr16_b64_v4i16((LAS s16x4*)p), b = __builtin_amdgcn_ds_read_tr16_b64_v4i16((LAS s16x4*)(p + 512));
    return __builtin_bit_cast(bf16x8, __builtin_shufflevector(a, b, 0, 1, 2, 3, 4, 5, 6, 7));
}
DEVI void swap16(unsigned& a, unsigned& b) { auto r = __builtin_amdgcn_permlane16_swap(a, b, false, false); a = r[0]; b = r[1]; }
DEVI void swap32(unsigned& a, unsigned& b) { auto r = __builtin_amdgcn_permlane32_swap(a, b, false, false); a = r[0]; b = r[1]; }
DEVI float sum_x16(float x) { unsigned a = __float_as_uint(x), b = a; swap16(a, b); return __uint_as_float(a) + __uint_as_float(b); }
DEVI float sum_x32(float x) { unsigned a = __float_as_uint(x), b = a; swap32(a, b); return __uint_as_float(a) + __uint_as_float(b); }
DEVI float max_x16(float x) { unsigned a = __float_as_uint(x), b = a; swap16(a, b); return fmaxf(__uint_as_float(a), __uint_as_float(b)); }
DEVI float max_x32(float x) { unsigned a = __float_as_uint(x), b = a; swap32(a, b); return fmaxf(__uint_as_float(a), __uint_as_float(b)); }
template <int C> DEVI float dppf(float x) { return __int_as_float(__builtin_amdgcn_mov_dpp(__float_as_int(x), C, 0xf, 0xf, true)); }
DEVI float nanmax(float a, float b) { return (a > b || a != a) ? a : b; }
DEVI float row_nanmax(float x) { x = nanmax(x, dppf<0x128>(x)); x = nanmax(x, dppf<0x124>(x)); x = nanmax(x, dppf<0x122>(x)); return nanmax(x, dppf<0x121>(x)); }
DEVI float row_sum(float x) { x += dppf<0x128>(x); x += dppf<0x124>(x); x += dppf<0x122>(x); return x + dppf<0x121>(x); }
DEVI void dma16(rsrc_t r, unsigned lds_addr, unsigned voff, unsigned soff) {
    asm volatile("s_mov_b32 m0, %0\n\ts_nop 0\n\tbuffer_load_dwordx4 %1, %2, %3 offen lds" :: "s"(lds_addr), "v"(voff), "s"(r), "s"(soff) : "m0", "memory"); }
DEVI unsigned short bld2(rsrc_t r, unsigned voff) { return __builtin_amdgcn_raw_buffer_load_b16(r, voff, 0, 0); }
DEVI void wg_barrier() { asm volatile("s_waitcnt lgkmcnt(0)\n\ts_barrier" ::: "memory"); }
DEVI void drain_vm() { asm volatile("s_waitcnt vmcnt(0)" ::: "memory"); }
DEVI unsigned long long mix64(unsigned long long z) {
    z += 0x9E3779B97F4A7C15ull; z = (z ^ (z >> 30)) * 0xBF58476D1CE4E5B9ull; z = (z ^ (z >> 27)) * 0x94D049BB133111EBull; return z ^ (z >> 31); }
DEVI void wait_tag(gu64* word, unsigned long long tag) {
    for (unsigned spins = 0; spins < SPIN_CAP; ++spins) {
        const unsigned long long v = __hip_atomic_load(word, RLX_AGENT);
        if (__all(v == tag)) break;
        __builtin_amdgcn_s_sleep(2);
    }
    asm volatile("" ::: "memory");
}
#define STAMP(i) do {} while (0)

DEVI f32x16 pv(bf16x8 v, f32x8 p, f32x16 o) {
    return __builtin_amdgcn_smfmac_f32_32x32x32_bf16(v, __builtin_bit_cast(bf16x16, p), o, 0xDDDD, 0, 0); }
DEVI f32x8 lo8(const f32x16& s) { return __builtin_shufflevector(s, s, 0, 1, 2, 3, 4, 5, 6, 7); }
DEVI f32x8 hi8(const f32x16& s) { return __builtin_shufflevector(s, s, 8, 9, 10, 11, 12, 13, 14, 15); }
DEVI void put_partial(lptr p, int lane, const f32x16& o, float m) {
    *(LAS f32x4*)(p + lane * 16) = (f32x4){o[0], o[1], o[2], o[3]};
    *(LAS f32x4*)(p + 1024 + lane * 16) = (f32x4){o[4], o[5], o[6], o[7]};
    *(LAS float*)(p + 2048 + lane * 4) = lane >= 32 ? m : o[8];
}
DEVI void attn_block_shifted(const bf16x8 (&kf)[8], const bf16x8 (&vf)[8][2], bf16x8 qf, f32x16& o, float& m_out) {
    f32x16 c = {};
    float m = -INFINITY;
#pragma unroll
    for (int t = 0; t < 8; ++t) { const f32x16 s = mfma32(kf[t], qf, c);
#pragma unroll
        for (int r = 0; r < 16; ++r) m = fmaxf(m, s[r]); }
    m = max_x32(m);
#pragma unroll
    for (int r = 0; r < 16; ++r) c[r] = -m;
    o = f32x16{};
#pragma unroll
    for (int t = 0; t < 8; ++t) {
        f32x16 s = mfma32(kf[t], qf, c);
#pragma unroll
        for (int r = 0; r < 16; ++r) s[r] = __builtin_amdgcn_exp2f(s[r]);
        o = pv(vf[t][0], lo8(s), o); o = pv(vf[t][1], hi8(s), o);
    }
    m_out = m;
}
typedef unsigned u32x16 __attribute__((ext_vector_type(16)));
#define R(b, i) "v[" #b "+" #i "]"
#define RR(b, i, n) "v[" #b "+" #i ":" #b "+" #i "+" #n "]"
#define A_EX(c, i) "v_exp_f32 " R(c, i) ", " R(c, i) "\n\t"
#define A_PV(o, vi, p, pi) "v_smfmac_f32_32x32x32_bf16 " RR(o, 0, 15) ", " RR(192, vi, 3) ", " RR(p, pi, 7) ", v83\n\t"
#define A_QK(d, ki, q) "v_mfma_f32_32x32x16_bf16 " RR(d, 0, 15) ", " RR(160, ki, 3) ", " RR(q, 0, 3) ", 0\n\t"
#define A_ZERO(o) "v_mfma_f32_32x32x16_bf16 " RR(o, 0, 15) ", v[84:87], v[84:87], 0\n\t"
#define A_TILE(c, p, oc, op, vprev, vcur, knext, q, X0, X1) \
    A_EX(c, 0) A_EX(c, 1) A_PV(op, vprev, p, 8) X0 A_EX(c, 2) A_EX(c, 3) A_EX(c, 4) A_QK(p, knext, q) \
    A_EX(c, 5) A_EX(c, 6) A_EX(c, 7) A_EX(c, 8) A_EX(c, 9) A_EX(c, 10) A_PV(oc, vcur, c, 0) X1 A_EX(c, 11) A_EX(c, 12) A_EX(c, 13) A_EX(c, 14) A_EX(c, 15)
#define A_PUT(o, a16, a4) "ds_write_b128 " a16 ", " RR(o, 0, 3) "\n\tds_write_b128 " a16 ", " RR(o, 4, 3) " offset:1024\n\tds_write_b32 " a4 ", " R(o, 8) " offset:2048\n\t"
#define A_BLOCK(o, op, qc, qn, qoff, a16, a4) \
    A_TILE(128, 144, o, op, 60, 0, 4, qc, A_ZERO(o), "ds_read_b128 " RR(qn, 0, 3) ", v82 offset:" #qoff "\n\t") \
    A_TILE(144, 128, o, o, 4, 8, 8, qc, "", A_PUT(op, a16, a4)) \
    A_TILE(128, 144, o, o, 12, 16, 12, qc, "", "") A_TILE(144, 128, o, o, 20, 24, 16, qc, "", "") \
    A_TILE(128, 144, o, o, 28, 32, 20, qc, "", "") A_TILE(144, 128, o, o, 36, 40, 24, qc, "", "") \
    A_TILE(128, 144, o, o, 44, 48, 28, qc, "s_waitcnt lgkmcnt(0)\n\t", "") A_TILE(144, 128, o, o, 52, 56, 0, qn, "", "")
#define CL8(a, b, c, d, e, f, g, h) "v" #a, "v" #b, "v" #c, "v" #d, "v" #e, "v" #f, "v" #g, "v" #h
DEVI void attn_fast(u32x16 k0, u32x16 k1, u32x16 v0, u32x16 v1, u32x16 v2, u32x16 v3, unsigned qaddr, unsigned p16, unsigned p4) {
    unsigned cnt, pa16 = p16 + 7 * 8 * P_SZ, pa4 = p4 + 7 * 8 * P_SZ;
    asm volatile(
        "ds_read_b128 v[88:91], v82\n\t"
        "v_mov_b32 v83, 0xdddd\n\tv_mov_b32 v84, 0\n\tv_mov_b32 v85, 0\n\tv_mov_b32 v86, 0\n\tv_mov_b32 v87, 0\n\t"
        "s_mov_b32 %0, 4\n\t"
        "s_waitcnt lgkmcnt(0)\n\t"
        A_QK(128, 0, 88)
        "s_nop 11\n"
        "1:\n\t"
        A_BLOCK(96, 112, 88, 92, 1024, "v81", "v79")
        A_BLOCK(112, 96, 92, 88, 2048, "v80", "v78")
        "v_add_u32 v82, 0x800, v82\n\t"
        "v_add_u32 v81, 0x4800, v80\n\tv_add_u32 v79, 0x4800, v78\n\t"
        "v_add_u32 v80, 0x9000, v80\n\tv_add_u32 v78, 0x9000, v78\n\t"
        "s_sub_u32 %0, %0, 1\n\ts_cmp_lg_u32 %0, 0\n\ts_cbranch_scc1 1b\n\t"
        A_PV(112, 60, 144, 8)
        "s_nop 15\n\t"
        A_PUT(112, "v81", "v79")
        "s_waitcnt lgkmcnt(0)"
        : "=&s"(cnt), "+{v82}"(qaddr), "+{v81}"(pa16), "+{v80}"(p16), "+{v79}"(pa4), "+{v78}"(p4)
        : "{v[160:175]}"(k0), "{v[176:191]}"(k1), "{v[192:207]}"(v0), "{v[208:223]}"(v1), "{v[224:239]}"(v2), "{v[240:255]}"(v3)
        : "memory", "scc", CL8(83, 84, 85, 86, 87, 88, 89, 90), CL8(91, 92, 93, 94, 95, 96, 97, 98), CL8(99, 100, 101, 102, 103, 104, 105, 106),
          CL8(107, 108, 109, 110, 111, 112, 113, 114), CL8(115, 116, 117, 118, 119, 120, 121, 122), CL8(123, 124, 125, 126, 127, 128, 129, 130),
          CL8(131, 132, 133, 134, 135, 136, 137, 138), CL8(139, 140, 141, 142, 143, 144, 145, 146), CL8(147, 148, 149, 150, 151, 152, 153, 154),
          "v155", "v156", "v157", "v158", "v159");
}
static_assert(8 * P_SZ == 0x4800, "partial stride");
DEVI u32x16 cat4(u32x4 a, u32x4 b, u32x4 c, u32x4 d) {
    return (u32x16){a[0], a[1], a[2], a[3], b[0], b[1], b[2], b[3], c[0], c[1], c[2], c[3], d[0], d[1], d[2], d[3]}; }
DEVI u32x4 asu(bf16x8 v) { return __builtin_bit_cast(u32x4, v); }

__global__ __launch_bounds__(512, 2) void fused_kernel(
    const unsigned short* __restrict__ x, const unsigned short* __restrict__ wqkv, const unsigned short* __restrict__ wo,
    const unsigned short* __restrict__ u, const unsigned short* __restrict__ lar, const unsigned short* __restrict__ aim,
    const unsigned short* __restrict__ cre, const unsigned short* __restrict__ cim, const unsigned short* __restrict__ ldt,
    const unsigned short* __restrict__ dsk, unsigned short* __restrict__ out, char* __restrict__ ws_) {
    extern __shared__ __attribute__((aligned(16))) char smem_[];
    const lptr lds = (lptr)smem_;
    GAS char* ws = (GAS char*)ws_;
#pragma unroll 1
    for (int pass = 0; pass < NPASS; ++pass) {
    int tid_ = threadIdx.x, bid_ = blockIdx.x; asm volatile("" : "+v"(tid_), "+s"(bid_));
    const int tid = tid_, lane = tid & 63, w = __builtin_amdgcn_readfirstlane(tid >> 6);
    const int r16 = lane & 15, g = lane >> 4, l31 = lane & 31, hh = lane >> 5;
    const int xcd = bid_ & 7, sl = bid_ >> 3, h = 4 * xcd + (sl >> 3), j = sl & 7, d0 = 64 * xcd + 2 * sl;
    const rsrc_t rws = mk_rsrc(ws_, WS_USED);
    STAMP(0);
    unsigned long long nonce = __hip_atomic_load((gu64*)(ws + WS_NONCE), RLX_AGENT);
    unsigned long long tag;

    const int kch = (w >> 1) & 1, kn = tid & 127;
    unsigned short p_lar, p_aim, p_cre, p_cim, p_ldt;
    { const unsigned o = ((d0 + kch) * 128 + kn) * 2;
      p_lar = bld2(mk_rsrc(lar, 131072), o); p_aim = bld2(mk_rsrc(aim, 131072), o); p_cre = bld2(mk_rsrc(cre, 131072), o); p_cim = bld2(mk_rsrc(cim, 131072), o);
      p_ldt = bld2(mk_rsrc(ldt, 1024), (d0 + kch) * 2); }

    {
        const int hq = 4 * (xcd & 1) + (sl >> 3), rb = 8 * (xcd >> 1) + (sl & 7), koff = (sl & 7) + 2 * (sl >> 3);
        const rsrc_t rw = mk_rsrc(wqkv, 512 * 1536 * 2), rx = mk_rsrc(x, 2048 * 512 * 2);
        const unsigned lds0 = (unsigned)(uintptr_t)lds;
        const int rl = lane >> 3, c8 = lane & 7;
        const unsigned xv = rl * 1024 + ((c8 ^ rl) * 16);
        const unsigned wv = rl * 3072 + (((c8 >> 1) ^ (((rl >> 1) & 1) + 2 * (w & 1))) * 32) + (c8 & 1) * 16;
        auto issue = [&](int i) {
            const int kc = (i + koff) & 7; const unsigned dst = lds0 + L_RING + (i & 3) * SLOT + w * 1024;
            dma16(rx, dst, xv, (64 * rb + 8 * w) * 1024 + kc * 128);
            const unsigned ws0 = (64 * kc + 8 * w) * 3072 + hq * 128;
            dma16(rw, dst + 8192, wv, ws0); dma16(rw, dst + 16384, wv, ws0 + 1024); dma16(rw, dst + 24576, wv, ws0 + 2048); };
        issue(0); issue(1); issue(2);
        if (tid < 9) *(LAS unsigned*)(lds + L_MISC + (tid == 8 ? 32 : 64 + 4 * tid)) = 0u;
        const int tg = w & 1, hl = w >> 1, hp = 4 * hq + hl;
        const int xa = (32 * tg + r16) * 128 + ((g ^ (r16 & 7)) * 16);
        const int wa = 8192 + (8 * g + (r16 >> 2)) * 128 + ((hl ^ (((r16 >> 3) & 1) + 2 * (g & 1))) * 32) + (r16 & 3) * 8;
        f32x4 aq[2] = {}, ak[2] = {}, av[2] = {};
#pragma unroll 1
        for (int i = 0; i < 8; ++i) {
            if (i < 6) asm volatile("s_waitcnt vmcnt(8)" ::: "memory"); else if (i == 6) asm volatile("s_waitcnt vmcnt(4)" ::: "memory"); else drain_vm();
            wg_barrier();
            if (i == 0) STAMP(1);
            if (i < 5) issue(i + 3);
            const lptr sp = lds + L_RING + (i & 3) * SLOT;
#pragma unroll
            for (int ks = 0; ks < 2; ++ks) {
                const lptr wp = sp + wa + ks * 4096, xp = sp + (xa ^ (ks * 64));
                const bf16x8 fq = tr_frag(wp), fk = tr_frag(wp + 8192), fv = tr_frag(wp + 16384);
#pragma unroll
                for (int tb = 0; tb < 2; ++tb) { const bf16x8 xb = asb(lds16(xp + tb * 2048));
                    aq[tb] = mfma16(fq, xb, aq[tb]); ak[tb] = mfma16(fk, xb, ak[tb]); av[tb] = mfma16(xb, fv, av[tb]); }
            }
        }
        STAMP(2);
        asm volatile("" : "+v"(nonce));
        tag = mix64(nonce + pass);
        float q2 = 0.f, k2 = 0.f;
        u32x4 qw, kw, vw;
#pragma unroll
        for (int tb = 0; tb < 2; ++tb) {
            const f32x4 q = aq[tb] * QSCALE, k = ak[tb], v = av[tb];
            q2 = nanmax(q2, sum_x32(sum_x16(q[0] * q[0] + q[1] * q[1] + q[2] * q[2] + q[3] * q[3])));
            k2 = nanmax(k2, sum_x32(sum_x16(k[0] * k[0] + k[1] * k[1] + k[2] * k[2] + k[3] * k[3])));
            qw[2 * tb] = pk2(q[0], q[1]); qw[2 * tb + 1] = pk2(q[2], q[3]);
            kw[2 * tb] = pk2(k[0], k[1]); kw[2 * tb + 1] = pk2(k[2], k[3]);
            vw[2 * tb] = pk2(v[0], v[1]); vw[2 * tb + 1] = pk2(v[2], v[3]);
        }
        auto sw = [](u32x4 a) { unsigned a0 = a[0], a1 = a[1], b0 = a[2], b1 = a[3]; swap16(a0, b0); swap16(a1, b1); return (u32x4){a0, a1, b0, b1}; };
        qw = sw(qw); kw = sw(kw); vw = sw(vw);
        q2 = row_nanmax(q2); k2 = row_nanmax(k2);
        const int tile = (hp * 8 + (rb >> 2)) * 8 + 2 * (rb & 3) + tg;
        const unsigned kvb = WS_QKV + tile * 3072;
        const unsigned kpos = (g >> 1) * 512 + (16 * (g & 1) + r16) * 16;
        bst16_sc1(qw, rws, kpos, kvb);
        bst16_sc1(kw, rws, kpos, kvb + 1024);
        bst16_sc1(vw, rws, (g & 1) * 512 + (g >> 1) * 256 + r16 * 16, kvb + 2048);
        if (lane < 2) __hip_atomic_store((GAS unsigned*)(ws + WS_NRM) + tile * 2 + lane, __float_as_uint(lane ? k2 : q2), RLX_AGENT);
        drain_vm();
        wg_barrier();
        if (tid == 0) __hip_atomic_store((gu64*)(ws + WS_FLAG_P + (hq * 32 + rb) * 128), tag, RLX_AGENT);
        STAMP(3);
    }

    {
        const int role = w >> 2;
        const float dt = rbf(expf(bf2f(p_ldt)));
        const float zr = -rbf(expf(bf2f(p_lar))) * dt, zi = bf2f(p_aim) * dt;
        const f32x2 c = {2.f * bf2f(p_cre) * dt, 2.f * bf2f(p_cim) * dt};
        const float zl = zr * 1.4426950408889634f, zt = zi * 0.15915494309189535f;
        auto cexp = [&](float p) { const float mag = __builtin_amdgcn_exp2f(zl * p), a = __builtin_amdgcn_fractf(zt * p);
            return (f32x2){mag * __builtin_amdgcn_cosf(a), mag * __builtin_amdgcn_sinf(a)}; };
        auto cmul = [](f32x2 a, f32x2 fr, f32x2 fi) { return a * fr + (f32x2){a[1], a[0]} * fi; };
        const f32x2 e = cexp(32.f), er = {e[0], e[0]}, ei = {-e[1], e[1]};
        const lptr xp = lds + L_XY + kch * XY_CH + kn * 4;
        if (role == 0) {
            f32x2 a = c;
#pragma unroll
            for (int i = 0; i < 48; ++i) { *(LAS unsigned*)(xp + i * XY_ROW) = pk2(a[0], a[1]); a = cmul(a, er, ei); }
        } else {
            const f32x2 s = cexp(1536.f), f = cexp(1.f), fr = {f[0], f[0]}, fi = {f[1], -f[1]};
            f32x2 a = cmul(c, (f32x2){s[0], s[0]}, (f32x2){-s[1], s[1]}), y = {1.f, 0.f};
#pragma unroll
            for (int i = 0; i < 32; ++i) {
                if (i < 16) { *(LAS unsigned*)(xp + (48 + i) * XY_ROW) = pk2(a[0], a[1]); a = cmul(a, er, ei); }
                *(LAS unsigned*)(xp + (64 + i) * XY_ROW) = pk2(y[0], y[1]); y = cmul(y, fr, fi);
            }
            const float sm = sum_x32(sum_x16(row_sum(a[0])));
            if (lane == 0) *(LAS float*)(lds + L_MISC + 48 + 4 * (w & 3)) = sm;
        }
        wg_barrier();
        STAMP(4);
    }

    bf16x8 kf[8], vf[8][2];
    float k2w;
    {
        wait_tag((gu64*)(ws + WS_FLAG_P + ((h >> 2) * 32 + (lane == 4 ? 4 * j + (w >> 1) : 4 * w + (lane & 3))) * 128), tag);
        STAMP(5);
        const unsigned kvb = WS_QKV + (h * 8 + w) * 8 * 3072;
        const unsigned vpos = ((lane >> 4) & 1) * 512 + hh * 256 + r16 * 16;
        const u32x4 qv = bld16_sc1(rws, lane * 16, WS_QKV + ((h * 8 + j) * 8 + w) * 3072);
        const unsigned q2v = __builtin_amdgcn_raw_buffer_load_b32(rws, 0, WS_NRM + ((h * 8 + j) * 8 + w) * 8, AUX_SC1);
        k2w = __uint_as_float(__builtin_amdgcn_raw_buffer_load_b32(rws, (lane & 7) * 8 + 4, WS_NRM + (h * 8 + w) * 64, AUX_SC1));
        u32x4 vr[8];
#pragma unroll
        for (int t = 0; t < 8; ++t) { kf[t] = asb(bld16_sc1(rws, lane * 16 + 1024, kvb + t * 3072)); vr[t] = bld16_sc1(rws, vpos + 2048, kvb + t * 3072); }

        {
            const int ch = w >> 2, mb = w & 3;
            const lptr ap = lds + L_XY + ch * XY_CH + (16 * mb + r16) * XY_ROW + g * 16;
            const lptr bp = lds + L_XY + ch * XY_CH + (64 + r16) * XY_ROW + g * 16;
            f32x4 a0 = {}, a1 = {};
#pragma unroll
            for (int ks = 0; ks < 8; ++ks) { const bf16x8 a = asb(lds16(ap + ks * 64));
                a0 = mfma16(a, asb(lds16(bp + ks * 64)), a0); a1 = mfma16(a, asb(lds16(bp + 16 * XY_ROW + ks * 64)), a1); }
            const lptr kp = lds + L_KD + ch * KD_CH + ((16 * mb + 4 * g) * 32 + r16) * 2;
#pragma unroll
            for (int i = 0; i < 4; ++i) { const unsigned v = pk2(a0[i], a1[i]);
                *(LAS unsigned short*)(kp + i * 64) = (unsigned short)v; *(LAS unsigned short*)(kp + i * 64 + 32) = (unsigned short)(v >> 16); }
            if (lane == 0 && mb == 0) { const float s = *(LAS float*)(lds + L_MISC + 48 + 8 * ch) + *(LAS float*)(lds + L_MISC + 52 + 8 * ch);
                *(LAS unsigned short*)(lds + L_KD + ch * KD_CH + 4096) = (unsigned short)pk2(s, 0.f); }
        }
        sts16(lds + L_Q + w * 1024 + lane * 16, qv);
        if (lane == 0) *(LAS unsigned*)(lds + L_MISC + 4 * w) = q2v;
        wg_barrier();
#pragma unroll
        for (int t = 0; t < 8; ++t) {
            u32x4 one = {0x3F803F80u, 0x3F803F80u, 0x3F803F80u, 0x3F803F80u};
            unsigned a[4], b[4];
#pragma unroll
            for (int i = 0; i < 4; ++i) { a[i] = vr[t][i]; b[i] = one[i]; swap16(a[i], b[i]); }
            vf[t][0] = asb((u32x4){a[0], a[1], a[2], a[3]}); vf[t][1] = asb((u32x4){b[0], b[1], b[2], b[3]});
        }
    }
    unsigned uu[4];
    unsigned kr[8];
    {
        const int ch = tid >> 8, y = tid & 255;
        const lptr kp = lds + L_KD + ch * KD_CH;
#pragma unroll
        for (int i = 0; i < 8; ++i) {
            const int l0 = 2047 - 8 * y - 2 * i, l1 = l0 - 1;
            const unsigned pp = pk2((float)l0, (float)l1);
            const int p0 = (int)bf_lo(pp), p1 = (int)bf_hi(pp);
            const unsigned v0 = l0 >= 0 ? *(LAS unsigned short*)(kp + 2 * max(p0, 0)) : 0u, v1 = l1 >= 0 ? *(LAS unsigned short*)(kp + 2 * max(p1, 0)) : 0u;
            kr[i] = v0 | (v1 << 16);
        }
        wg_barrier();
        STAMP(6);
    }

    {
        float q2 = *(LAS float*)(lds + L_MISC + 4 * (lane & 7));
        q2 = nanmax(q2, dppf<0x124>(q2)); q2 = nanmax(q2, dppf<0x122>(q2)); q2 = nanmax(q2, dppf<0x121>(q2));
        k2w = nanmax(k2w, dppf<0x124>(k2w)); k2w = nanmax(k2w, dppf<0x122>(k2w)); k2w = nanmax(k2w, dppf<0x121>(k2w));
        const bool fast = __builtin_amdgcn_readfirstlane((q2 * k2w * 1.05f <= SAFE2) ? 1 : 0) != 0;
        const lptr qp = lds + L_Q + lane * 16;
        const lptr pp = lds + L_P + w * P_SZ;
#pragma unroll
        for (int i = 0; i < 4; ++i) uu[i] = __builtin_amdgcn_raw_buffer_load_b32(mk_rsrc(u, 2048 * 1024), (4 * tid + i) * 1024, d0 * 2, 0);
        if (__builtin_expect(fast, 1)) {
            const unsigned l0 = (unsigned)(uintptr_t)lds;
            attn_fast(cat4(asu(kf[0]), asu(kf[1]), asu(kf[2]), asu(kf[3])), cat4(asu(kf[4]), asu(kf[5]), asu(kf[6]), asu(kf[7])),
                      cat4(asu(vf[0][0]), asu(vf[0][1]), asu(vf[1][0]), asu(vf[1][1])), cat4(asu(vf[2][0]), asu(vf[2][1]), asu(vf[3][0]), asu(vf[3][1])),
                      cat4(asu(vf[4][0]), asu(vf[4][1]), asu(vf[5][0]), asu(vf[5][1])), cat4(asu(vf[6][0]), asu(vf[6][1]), asu(vf[7][0]), asu(vf[7][1])),
                      l0 + L_Q + lane * 16, l0 + L_P + w * P_SZ + lane * 16, l0 + L_P + w * P_SZ + lane * 4);
        } else {
            if (lane == 0) { *(LAS unsigned*)(lds + L_MISC + 32) = 1u; *(LAS unsigned*)(lds + L_MISC + 64 + 4 * w) = 1u; }
#pragma unroll 1
            for (int qb = 0; qb < 8; ++qb) {
                f32x16 o; float m;
                attn_block_shifted(kf, vf, asb(lds16(qp + qb * 1024)), o, m);
                put_partial(pp + qb * (8 * P_SZ), lane, o, m);
            }
        }
        STAMP(7);
        wg_barrier();
        STAMP(8);
        const lptr p = lds + L_P + w * (8 * P_SZ);
        f32x4 oa = {}, ob = {}; float l = 0.f;
        if (*(LAS unsigned*)(lds + L_MISC + 32) == 0u) {
#pragma unroll
            for (int i = 0; i < 8; ++i) { oa += *(LAS f32x4*)(p + i * P_SZ + lane * 16); ob += *(LAS f32x4*)(p + i * P_SZ + 1024 + lane * 16);
                l += *(LAS float*)(p + i * P_SZ + 2048 + l31 * 4); }
        } else {
            float mi[8], mx = -INFINITY;
#pragma unroll
            for (int i = 0; i < 8; ++i) { mi[i] = *(LAS unsigned*)(lds + L_MISC + 64 + 4 * i) ? *(LAS float*)(p + i * P_SZ + 2048 + 128 + l31 * 4) : 0.f; mx = fmaxf(mx, mi[i]); }
#pragma unroll
            for (int i = 0; i < 8; ++i) { const float f = __builtin_amdgcn_exp2f(mi[i] - mx);
                oa += *(LAS f32x4*)(p + i * P_SZ + lane * 16) * f; ob += *(LAS f32x4*)(p + i * P_SZ + 1024 + lane * 16) * f;
                l += *(LAS float*)(p + i * P_SZ + 2048 + l31 * 4) * f; }
        }
        const float rl = 1.f / l;
        oa *= rl; ob *= rl;
        unsigned a0 = pk2(oa[0], oa[1]), a1 = pk2(oa[2], oa[3]), b0 = pk2(ob[0], ob[1]), b1 = pk2(ob[2], ob[3]);
        swap32(a0, b0); swap32(a1, b1);
        const u32x4 ow = {a0, a1, b0, b1};
        bst16_sc1(ow, rws, (l31 >> 4) * 16384 + hh * 256 + (l31 & 15) * 16, WS_O + (16 * j + 2 * w) * 16384 + h * 512);
        wg_barrier();
    }

    {
        const int ch = tid >> 8, y = tid & 255;
        unsigned sft[7];
#pragma unroll
        for (int i = 0; i < 7; ++i) sft[i] = __builtin_amdgcn_alignbit(kr[i + 1], kr[i], 16);
        const lptr kp = lds + L_KR + ch * KR_CH + y * 16;
#pragma unroll
        for (int c = 0; c < 4; ++c) {
            sts16(kp + (2 * c) * KR_CP, (u32x4){kr[c], kr[c + 1], kr[c + 2], kr[c + 3]});
            sts16(kp + (2 * c + 1) * KR_CP, (u32x4){sft[c], sft[c + 1], sft[c + 2], sft[c + 3]});
        }
        if (tid < 16) sts16(lds + L_KR + (tid >> 3) * KR_CH + (tid & 7) * KR_CP + 4096, (u32x4){0u, 0u, 0u, 0u});
        if (tid < 64) sts16(lds + L_U + (tid >> 5) * U_CH + (tid & 31) * 16, (u32x4){0u, 0u, 0u, 0u});
        const unsigned sel_lo = 0x05040100u, sel_hi = 0x07060302u;
        *(LAS u32x2*)(lds + L_U + 512 + tid * 8) = (u32x2){__builtin_amdgcn_perm(uu[1], uu[0], sel_lo), __builtin_amdgcn_perm(uu[3], uu[2], sel_lo)};
        *(LAS u32x2*)(lds + L_U + U_CH + 512 + tid * 8) = (u32x2){__builtin_amdgcn_perm(uu[1], uu[0], sel_hi), __builtin_amdgcn_perm(uu[3], uu[2], sel_hi)};
        drain_vm();
        wg_barrier();
        if (tid == 0) __hip_atomic_store((gu64*)(ws + WS_FLAG_O + (j * 32 + h) * 8), tag, RLX_AGENT);
        STAMP(9);
    }

    {
        const int ncol0 = 256 * (xcd >> 2) + 64 * (h & 3);
        const rsrc_t rwo = mk_rsrc(wo, 512 * 512 * 2);
        u32x4 wr[8];
        const unsigned wv = ((8 * ((lane >> 3) & 3) + 4 * hh + ((lane >> 1) & 3)) * 512 + 8 * (lane & 1)) * 2;
#pragma unroll
        for (int i = 0; i < 8; ++i) wr[i] = bld16(rwo, wv + (i & 3) * 32, (32 * (2 * w + (i >> 2)) * 512 + ncol0) * 2);

        const int ch = w >> 2, cs = (w >> 1) & 1, rg = w & 1;
        const lptr ab = lds + L_KR + ch * KR_CH + (7 - (r16 & 7)) * KR_CP + 2 * (2040 - 8 * (r16 >> 3) - 16 * (g >> 1) + 8 * (g & 1));
        const lptr bb = lds + L_U + ch * U_CH + 512 + 32 * (r16 - (g >> 1)) + 16 * (g & 1);
        f32x4 acc[4] = {};
        auto conv = [&](auto CS) {
            constexpr int cs_ = decltype(CS)::value;
            constexpr int C0 = cs_ ? 1 : 0, C1 = cs_ ? 2 : 3, C2 = cs_ ? 5 : 4, C3 = cs_ ? 6 : 7;
#pragma unroll 1
            for (int rr = 0; rr < 4; ++rr) {
                const int r = 4 * rg + rr;
                const lptr ap = ab - 64 * r - 3584, bp = bb - 64 * r;
                bf16x8 A[8], B[8];
#pragma unroll
                for (int m = 0; m <= C3; ++m) { A[m] = asb(lds16(ap + 512 * (7 - m))); B[m] = asb(lds16(bp + 512 * m)); }
#pragma unroll
                for (int m = 0; m <= C3; ++m) {
                    if (m <= C0) acc[0] = mfma16(A[m], B[C0 - m < 0 ? 0 : C0 - m], acc[0]);
                    if (m <= C1) acc[1] = mfma16(A[m], B[C1 - m < 0 ? 0 : C1 - m], acc[1]);
                    if (m <= C2) acc[2] = mfma16(A[m], B[C2 - m < 0 ? 0 : C2 - m], acc[2]);
                    acc[3] = mfma16(A[m], B[C3 - m], acc[3]);
                }
            }
        };
        if (cs) conv(std::integral_constant<int, 1>{}); else conv(std::integral_constant<int, 0>{});
#pragma unroll
        for (int i = 0; i < 8; ++i) sts16(lds + L_WO + ((2 * w + (i >> 2)) * 4 + (i & 3)) * 1024 + lane * 16, wr[i]);
        STAMP(10);
        wg_barrier();
        {
            const lptr cp = lds + L_CP + (ch * 2 + rg) * 8192 + (4 * r16 + g) * 16;
            const int c0 = cs ? 1 : 0, c1 = cs ? 2 : 3, c2 = cs ? 5 : 4, c3 = cs ? 6 : 7;
            *(LAS f32x4*)(cp + c0 * 1024) = acc[0]; *(LAS f32x4*)(cp + c1 * 1024) = acc[1];
            *(LAS f32x4*)(cp + c2 * 1024) = acc[2]; *(LAS f32x4*)(cp + c3 * 1024) = acc[3];
        }
        if (w == 0) wait_tag((gu64*)(ws + WS_FLAG_O + (j * 32 + l31) * 8), tag);
        wg_barrier();
        STAMP(11);
        if (pass == NPASS - 1 && bid_ == 0 && tid == 0) __hip_atomic_store((gu64*)(ws + WS_NONCE), tag, RLX_AGENT);
        const int rgp = w & 3, c2h = w >> 2, row0 = 256 * j + 64 * (xcd & 3) + 16 * rgp;
        u32x4 of[16];
        const unsigned ov = lane * 16;
#pragma unroll
        for (int ks = 0; ks < 16; ++ks) of[ks] = bld16_sc1(rws, ov + ks * 1024, WS_O + (row0 >> 4) * 16384);
        {
            const lptr cp = lds + L_CP + tid * 16;
            const f32x4 y0 = *(LAS f32x4*)cp + *(LAS f32x4*)(cp + 8192), y1 = *(LAS f32x4*)(cp + 16384) + *(LAS f32x4*)(cp + 24576);
            const unsigned dd = *(const unsigned*)(dsk + d0);
            const float s0 = bf_lo(dd), s1 = bf_hi(dd);
#pragma unroll
            for (int i = 0; i < 4; ++i) {
                const unsigned yy = pk2(y0[i], y1[i]), us = pk2(bf_lo(uu[i]) * s0, bf_hi(uu[i]) * s1);
                *(unsigned*)(out + (4 * tid + i) * 1024 + 512 + d0) = pk2(bf_lo(yy) + bf_lo(us), bf_hi(yy) + bf_hi(us));
            }
        }
        f32x4 p0 = {}, p1 = {};
        const lptr wp = lds + L_WO + (2 * c2h) * 1024 + lane * 8;
#pragma unroll
        for (int ks = 0; ks < 16; ++ks) { const bf16x8 b = asb(of[ks]);
            p0 = mfma16(tr_frag(wp + ks * 4096), b, p0); p1 = mfma16(tr_frag(wp + ks * 4096 + 1024), b, p1); }
        unsigned a0 = pk2(p0[0], p0[1]), a1 = pk2(p0[2], p0[3]), b0 = pk2(p1[0], p1[1]), b1 = pk2(p1[2], p1[3]);
        swap16(a0, b0); swap16(a1, b1);
        *(u32x4*)(out + (row0 + r16) * 1024 + ncol0 + 32 * c2h + 16 * (g & 1) + 8 * (g >> 1)) = (u32x4){a0, a1, b0, b1};
        STAMP(12);
    }
    wg_barrier();
    }
}

extern "C" void kernel_launch(void* const* d_in, const int* in_sizes, int n_in, void* d_out, int out_size, void* d_ws, size_t ws_size, hipStream_t stream) {
    (void)in_sizes; (void)n_in; (void)out_size; (void)ws_size;
    (void)hipFuncSetAttribute((const void*)fused_kernel, hipFuncAttributeMaxDynamicSharedMemorySize, LDS_BYTES);
    typedef const unsigned short* cp;
    fused_kernel<<<256, 512, LDS_BYTES, stream>>>((cp)d_in[0], (cp)d_in[1], (cp)d_in[2], (cp)d_in[3], (cp)d_in[4], (cp)d_in[5], (cp)d_in[6], (cp)d_in[7],
                                                  (cp)d_in[8], (cp)d_in[9], (unsigned short*)d_out, (char*)d_ws);
}
```

```cpp
#include <hip/hip_runtime.h>
#include <hip/hip_bf16.h>
#include <stdint.h>

#define GAS __attribute__((address_space(1)))
#define LAS __attribute__((address_space(3)))
#define DEVI __device__ __forceinline__
typedef __bf16 bf16x2 __attribute__((ext_vector_type(2)));
typedef __bf16 bf16x8 __attribute__((ext_vector_type(8)));
typedef __bf16 bf16x16 __attribute__((ext_vector_type(16)));
typedef float f32x2 __attribute__((ext_vector_type(2)));
typedef float f32x4 __attribute__((ext_vector_type(4)));
typedef float f32x8 __attribute__((ext_vector_type(8)));
typedef float f32x16 __attribute__((ext_vector_type(16)));
typedef unsigned u32x2 __attribute__((ext_vector_type(2)));
typedef unsigned u32x4 __attribute__((ext_vector_type(4)));
typedef short s16x2 __attribute__((ext_vector_type(2)));
typedef short s16x4 __attribute__((ext_vector_type(4)));
typedef short s16x8 __attribute__((ext_vector_type(8)));
typedef GAS unsigned long long gu64;
typedef LAS char* lptr;
typedef __amdgpu_buffer_rsrc_t rsrc_t;

constexpr unsigned WS_NONCE = 0;
constexpr unsigned WS_FLAG_P = 4096;
constexpr unsigned WS_FLAG_O = WS_FLAG_P + 256 * 128;
constexpr unsigned WS_FLAG_U = WS_FLAG_O + 2048;
constexpr unsigned WS_FLAG_Y = WS_FLAG_U + 2048;
constexpr unsigned WS_NRM = 49152;
constexpr unsigned TS = 3200;
constexpr unsigned WS_QKV = 131072;
constexpr unsigned WS_O = WS_QKV + 32 * 64 * TS;
constexpr unsigned WS_UT = WS_O + 2048 * 512 * 2;
constexpr unsigned WS_YT = WS_UT + 512 * 4096;
constexpr unsigned WS_USED = WS_YT + 512 * 4096;
constexpr unsigned WS_DBG = 16u << 20;

constexpr int L_RING = 0, SLOT = 32768;
constexpr int L_T1 = 131072, L_T2 = 139264;
constexpr int L_XY = 0, XY_ROW = 528, XY_CH = 96 * XY_ROW;
constexpr int L_KD = 2 * XY_CH, KD_CH = 4128;
constexpr int L_KR = 109632, KR_CP = 4160, KR_CH = 4 * KR_CP;
constexpr int L_U = 73728, U_CH = 4608;
constexpr int P_SZ = 2304, L_PB = 0, L_PA = 73728;
constexpr int L_WO = 0;
constexpr int L_PX = 65536;
constexpr int L_Q = 147456;
constexpr int L_MISC = 155648;
constexpr int LDS_BYTES = 163840;
static_assert(L_KD + 2 * KD_CH <= L_KR && L_KR + 2 * KR_CH <= L_Q && L_U + 2 * U_CH <= L_KD && L_PB + 32 * P_SZ <= L_PA && L_PA + 32 * P_SZ <= L_Q, "LDS map");

constexpr float QSCALE = 0.25f * 1.4426950408889634f;
constexpr float SAFE2 = 64.f * 64.f;
constexpr unsigned SPIN_CAP = 1u << 18;
constexpr int AUX_SC1 = 16;

#define SBAR() __builtin_amdgcn_sched_barrier(0)
#define RLX_AGENT __ATOMIC_RELAXED, __HIP_MEMORY_SCOPE_AGENT

DEVI rsrc_t mk_rsrc(const void* p, unsigned bytes) { return __builtin_amdgcn_make_buffer_rsrc((void*)p, (short)0, (int)bytes, 0x00020000); }
DEVI u32x4 bld16(rsrc_t r, unsigned voff, unsigned soff) { return __builtin_amdgcn_raw_buffer_load_b128(r, voff, soff, 0); }
DEVI u32x4 bld16_sc1(rsrc_t r, unsigned voff, unsigned soff) { return __builtin_amdgcn_raw_buffer_load_b128(r, voff, soff, AUX_SC1); }
DEVI void bst16_sc1(u32x4 v, rsrc_t r, unsigned voff, unsigned soff) { __builtin_amdgcn_raw_buffer_store_b128(v, r, voff + soff, 0, AUX_SC1); }
DEVI unsigned pk2(float lo, float hi) { f32x2 v = {lo, hi}; return __builtin_bit_cast(unsigned, __builtin_convertvector(v, bf16x2)); }
DEVI float bf_lo(unsigned v) { return __uint_as_float(v << 16); }
DEVI float bf_hi(unsigned v) { return __uint_as_float(v & 0xffff0000u); }
DEVI float bf2f(unsigned short v) { return __uint_as_float((unsigned)v << 16); }
DEVI float rbf(float x) { return bf_lo(pk2(x, 0.f)); }
DEVI bf16x8 asb(u32x4 v) { return __builtin_bit_cast(bf16x8, v); }
DEVI f32x4 mfma16(bf16x8 a, bf16x8 b, f32x4 c) { return __builtin_amdgcn_mfma_f32_16x16x32_bf16(a, b, c, 0, 0, 0); }
DEVI f32x16 mfma32(bf16x8 a, bf16x8 b, f32x16 c) { return __builtin_amdgcn_mfma_f32_32x32x16_bf16(a, b, c, 0, 0, 0); }
DEVI u32x4 lds16(lptr p) { return *(LAS u32x4*)p; }
DEVI void sts16(lptr p, u32x4 v) { *(LAS u32x4*)p = v; }
DEVI bf16x8 tr_frag(lptr p) {
    const s16x4 a = __builtin_amdgcn_ds_read_tr16_b64_v4i16((LAS s16x4*)p), b = __builtin_amdgcn_ds_read_tr16_b64_v4i16((LAS s16x4*)(p + 512));
    return __builtin_bit_cast(bf16x8, __builtin_shufflevector(a, b, 0, 1, 2, 3, 4, 5, 6, 7));
}
DEVI void swap16(unsigned& a, unsigned& b) { auto r = __builtin_amdgcn_permlane16_swap(a, b, false, false); a = r[0]; b = r[1]; }
DEVI void swap32(unsigned& a, unsigned& b) { auto r = __builtin_amdgcn_permlane32_swap(a, b, false, false); a = r[0]; b = r[1]; }
DEVI float sum_x16(float x) { unsigned a = __float_as_uint(x), b = a; swap16(a, b); return __uint_as_float(a) + __uint_as_float(b); }
DEVI float sum_x32(float x) { unsigned a = __float_as_uint(x), b = a; swap32(a, b); return __uint_as_float(a) + __uint_as_float(b); }
DEVI float max_x16(float x) { unsigned a = __float_as_uint(x), b = a; swap16(a, b); return fmaxf(__uint_as_float(a), __uint_as_float(b)); }
DEVI float max_x32(float x) { unsigned a = __float_as_uint(x), b = a; swap32(a, b); return fmaxf(__uint_as_float(a), __uint_as_float(b)); }
template <int C> DEVI float dppf(float x) { return __int_as_float(__builtin_amdgcn_mov_dpp(__float_as_int(x), C, 0xf, 0xf, true)); }
DEVI float nanmax(float a, float b) { return (a > b || a != a) ? a : b; }
DEVI float row_nanmax(float x) { x = nanmax(x, dppf<0x128>(x)); x = nanmax(x, dppf<0x124>(x)); x = nanmax(x, dppf<0x122>(x)); return nanmax(x, dppf<0x121>(x)); }
DEVI float row_max(float x) { x = fmaxf(x, dppf<0x128>(x)); x = fmaxf(x, dppf<0x124>(x)); x = fmaxf(x, dppf<0x122>(x)); return fmaxf(x, dppf<0x121>(x)); }
DEVI float row_sum(float x) { x += dppf<0x128>(x); x += dppf<0x124>(x); x += dppf<0x122>(x); return x + dppf<0x121>(x); }
DEVI void dma16(rsrc_t r, unsigned lds_addr, unsigned voff, unsigned soff) {
    asm volatile("s_mov_b32 m0, %0\n\ts_nop 0\n\tbuffer_load_dwordx4 %1, %2, %3 offen lds" :: "s"(lds_addr), "v"(voff), "s"(r), "s"(soff) : "m0", "memory"); }
template <int N> DEVI void wait_vm() { asm volatile("s_waitcnt vmcnt(%0)" :: "n"(N) : "memory"); }
DEVI void wg_barrier() { asm volatile("s_waitcnt lgkmcnt(0)\n\ts_barrier" ::: "memory"); }
DEVI void drain_vm() { asm volatile("s_waitcnt vmcnt(0)" ::: "memory"); }
DEVI unsigned long long mix64(unsigned long long z) {
    z += 0x9E3779B97F4A7C15ull; z = (z ^ (z >> 30)) * 0xBF58476D1CE4E5B9ull; z = (z ^ (z >> 27)) * 0x94D049BB133111EBull; return z ^ (z >> 31); }
DEVI void wait_tag(gu64* word, unsigned long long tag) {
#pragma unroll 1
    for (unsigned spins = 0; spins < SPIN_CAP; ++spins) {
        const unsigned long long v = __hip_atomic_load(word, RLX_AGENT);
        if (__all(v == tag)) break;
        __builtin_amdgcn_s_sleep(2);
    }
    asm volatile("" ::: "memory");
}
DEVI void wait_tag_fast(gu64* word, unsigned long long tag) {
    unsigned long long a = __hip_atomic_load(word, RLX_AGENT); __builtin_amdgcn_s_sleep(3);
    unsigned long long b = __hip_atomic_load(word, RLX_AGENT); __builtin_amdgcn_s_sleep(3);
    unsigned long long c = __hip_atomic_load(word, RLX_AGENT);
#pragma unroll 1
    for (unsigned spins = 0; spins < SPIN_CAP; ++spins) {
        if (__all(a == tag)) break;
        a = b; b = c; __builtin_amdgcn_s_sleep(3); c = __hip_atomic_load(word, RLX_AGENT);
    }
    asm volatile("" ::: "memory");
}
DEVI void grp_arrive(lptr c, int lane) { asm volatile("s_waitcnt lgkmcnt(0)" ::: "memory");
    if (lane == 0) __hip_atomic_fetch_add((LAS unsigned*)c, 1u, __ATOMIC_RELAXED, __HIP_MEMORY_SCOPE_WORKGROUP); }
DEVI void grp_wait(lptr c, unsigned n) {
#pragma unroll 1
    for (unsigned spins = 0; spins < (1u << 22); ++spins) { if ((unsigned)__builtin_amdgcn_readfirstlane(*(volatile LAS unsigned*)c) >= n) break; __builtin_amdgcn_s_sleep(1); }
    asm volatile("" ::: "memory"); }
DEVI unsigned long long peek(gu64* word) { const unsigned long long v = __hip_atomic_load(word, RLX_AGENT); SBAR(); return v; }
DEVI void confirm(unsigned long long v, gu64* word, unsigned long long tag) { if (!__all(v == tag)) wait_tag(word, tag); asm volatile("" ::: "memory"); }
#define STAMP(i) do {} while (0)
#define STAMP_DECL do {} while (0)
#define STAMP_FLUSH do {} while (0)

DEVI f32x16 pv(bf16x8 v, f32x8 p, f32x16 o) {
    return __builtin_amdgcn_smfmac_f32_32x32x32_bf16(v, __builtin_bit_cast(bf16x16, p), o, 0xDDDD, 0, 0); }
DEVI f32x8 lo8(const f32x16& s) { return __builtin_shufflevector(s, s, 0, 1, 2, 3, 4, 5, 6, 7); }
DEVI f32x8 hi8(const f32x16& s) { return __builtin_shufflevector(s, s, 8, 9, 10, 11, 12, 13, 14, 15); }
DEVI void put_partial(lptr p, int lane, const f32x16& o, float m) {
    *(LAS f32x4*)(p + lane * 16) = (f32x4){o[0], o[1], o[2], o[3]};
    *(LAS f32x4*)(p + 1024 + lane * 16) = (f32x4){o[4], o[5], o[6], o[7]};
    *(LAS float*)(p + 2048 + lane * 4) = lane >= 32 ? m : o[8];
}
DEVI void attn_block_shifted(rsrc_t rs, unsigned blk, unsigned vk, unsigned vv, bf16x8 qf, f32x16& o, float& m_out) {
    float m = -INFINITY;
#pragma unroll 1
    for (int t = 0; t < 8; ++t) { const f32x16 s = mfma32(asb(bld16_sc1(rs, vk + 1024, blk + t * TS)), qf, f32x16{});
#pragma unroll
        for (int r = 0; r < 16; ++r) m = fmaxf(m, s[r]); }
    m = max_x32(m);
    o = f32x16{};
#pragma unroll 1
    for (int t = 0; t < 8; ++t) {
        const bf16x8 v0 = asb(bld16_sc1(rs, vv + 2048, blk + t * TS)), v1 = asb(bld16_sc1(rs, vv + 2592, blk + t * TS));
        f32x16 s = mfma32(asb(bld16_sc1(rs, vk + 1024, blk + t * TS)), qf, f32x16{});
#pragma unroll
        for (int r = 0; r < 16; ++r) s[r] = __builtin_amdgcn_exp2f(s[r] - m);
        o = pv(v0, lo8(s), o); o = pv(v1, hi8(s), o);
    }
    m_out = m;
}
typedef unsigned u32x16 __attribute__((ext_vector_type(16)));
#define R(b, i) "v[" #b "+" #i "]"
#define RR(b, i, n) "v[" #b "+" #i ":" #b "+" #i "+" #n "]"
#define A_EX(c, i) "v_exp_f32 " R(c, i) ", " R(c, i) "\n\t"
#define A_PV(o, vi, p, pi) "v_smfmac_f32_32x32x32_bf16 " RR(o, 0, 15) ", " RR(192, vi, 3) ", " RR(p, pi, 7) ", v83\n\t"
#define A_QK(d, ki, q) "v_mfma_f32_32x32x16_bf16 " RR(d, 0, 15) ", " RR(160, ki, 3) ", " RR(q, 0, 3) ", 0\n\t"
#define A_ZERO(o) "v_mfma_f32_32x32x16_bf16 " RR(o, 0, 15) ", v[84:87], v[84:87], 0\n\t"
#define A_TILE(c, p, oc, op, vprev, vcur, knext, q, X0, X1, W) \
    A_EX(c, 0) A_EX(c, 1) A_PV(op, vprev, p, 8) X0 A_EX(c, 2) A_EX(c, 3) A_EX(c, 4) W A_QK(p, knext, q) \
    A_EX(c, 5) A_EX(c, 6) A_EX(c, 7) A_EX(c, 8) A_EX(c, 9) A_EX(c, 10) A_PV(oc, vcur, c, 0) X1 A_EX(c, 11) A_EX(c, 12) A_EX(c, 13) A_EX(c, 14) A_EX(c, 15)
#define A_PUT(o, a16, a4) "ds_write_b128 " a16 ", " RR(o, 0, 3) "\n\tds_write_b128 " a16 ", " RR(o, 4, 3) " offset:1024\n\tds_write_b32 " a4 ", " R(o, 8) " offset:2048\n\t"
#define A_BLOCK_W(o, op, qc, qn, qoff, a16, a4, W0, W1, W2, W3, W4, W5, W6, W7) \
    A_TILE(128, 144, o, op, 60, 0, 4, qc, A_ZERO(o), "ds_read_b128 " RR(qn, 0, 3) ", v82 offset:" #qoff "\n\t", W0) \
    A_TILE(144, 128, o, o, 4, 8, 8, qc, "", A_PUT(op, a16, a4), W1) \
    A_TILE(128, 144, o, o, 12, 16, 12, qc, "", "", W2) A_TILE(144, 128, o, o, 20, 24, 16, qc, "", "", W3) \
    A_TILE(128, 144, o, o, 28, 32, 20, qc, "", "", W4) A_TILE(144, 128, o, o, 36, 40, 24, qc, "", "", W5) \
    A_TILE(128, 144, o, o, 44, 48, 28, qc, "s_waitcnt lgkmcnt(0)\n\t", "", W6) A_TILE(144, 128, o, o, 52, 56, 0, qn, "", "", W7)
#define A_BLOCK(o, op, qc, qn, qoff, a16, a4) A_BLOCK_W(o, op, qc, qn, qoff, a16, a4, "", "", "", "", "", "", "", "")
#define VMW(n) "s_waitcnt vmcnt(" #n ")\n\t"
#define CL8(a, b, c, d, e, f, g, h) "v" #a, "v" #b, "v" #c, "v" #d, "v" #e, "v" #f, "v" #g, "v" #h
#define LD_TILE(t, so) "buffer_load_dwordx4 v[160+4*" #t ":163+4*" #t "], %[vk], %[rs], " so " offen offset:1024 sc1\n\t" \
    "buffer_load_dwordx4 v[192+8*" #t ":195+8*" #t "], %[vv], %[rs], " so " offen offset:2048 sc1\n\t" \
    "buffer_load_dwordx4 v[196+8*" #t ":199+8*" #t "], %[vv], %[rs], " so " offen offset:2592 sc1\n\t"
#define LD_ALL LD_TILE(0, "%[t0]") LD_TILE(1, "%[t1]") LD_TILE(2, "%[t2]") LD_TILE(3, "%[t3]") LD_TILE(4, "%[t4]") LD_TILE(5, "%[t5]") LD_TILE(6, "%[t6]") LD_TILE(7, "%[t7]")
#define KV_OUT [k0] "=&{v[160:175]}"(kv.k0), [k1] "=&{v[176:191]}"(kv.k1), [v0] "=&{v[192:207]}"(kv.v0), [v1] "=&{v[208:223]}"(kv.v1), [v2] "=&{v[224:239]}"(kv.v2), [v3] "=&{v[240:255]}"(kv.v3)
#define KV_TB [t0] "s"(tb[0]), [t1] "s"(tb[1]), [t2] "s"(tb[2]), [t3] "s"(tb[3]), [t4] "s"(tb[4]), [t5] "s"(tb[5]), [t6] "s"(tb[6]), [t7] "s"(tb[7])
struct KV { u32x16 k0, k1, v0, v1, v2, v3; };
DEVI void kv_request(KV& kv, unsigned& k2, rsrc_t rs, unsigned vk, unsigned vv, unsigned vn, unsigned sn, const unsigned (&tb)[8]) {
    asm volatile("buffer_load_dword %[k2], %[vn], %[rs], %[sn] offen sc1\n\t" LD_ALL VMW(24)
        : [k2] "=&v"(k2), KV_OUT : [rs] "s"(rs), [vk] "v"(vk), [vv] "v"(vv), [vn] "v"(vn), [sn] "s"(sn), KV_TB : "memory");
}
DEVI void kvq_request(KV& kv, unsigned& k2, u32x4& q0, u32x4& q1, unsigned& q2, rsrc_t rs, unsigned vk, unsigned vv, unsigned vn, unsigned sn,
                      unsigned vqn, unsigned sqn, unsigned sq, const unsigned (&tb)[8]) {
    asm volatile("buffer_load_dwordx4 %[q0], %[vk], %[rs], %[sq] offen sc1\n\t"
                 "buffer_load_dwordx4 %[q1], %[vk], %[rs], %[sq] offen offset:3200 sc1\n\t"
                 "buffer_load_dword %[q2], %[vqn], %[rs], %[sqn] offen sc1\n\t"
                 "buffer_load_dword %[k2], %[vn], %[rs], %[sn] offen sc1\n\t" LD_ALL VMW(24)
        : [q0] "=&v"(q0), [q1] "=&v"(q1), [q2] "=&v"(q2), [k2] "=&v"(k2), KV_OUT
        : [rs] "s"(rs), [vk] "v"(vk), [vv] "v"(vv), [vn] "v"(vn), [sn] "s"(sn), [vqn] "v"(vqn), [sqn] "s"(sqn), [sq] "s"(sq), KV_TB : "memory");
}
DEVI void kv_landed(KV& kv) {
    asm volatile("s_waitcnt vmcnt(0)" : "+{v[160:175]}"(kv.k0), "+{v[176:191]}"(kv.k1), "+{v[192:207]}"(kv.v0), "+{v[208:223]}"(kv.v1), "+{v[224:239]}"(kv.v2), "+{v[240:255]}"(kv.v3) :: "memory");
}
DEVI void attn_fast(const KV& kv, unsigned qaddr, unsigned p16, unsigned p4) {
    unsigned cnt, pa16 = p16 + 7 * 4 * P_SZ, pa4 = p4 + 7 * 4 * P_SZ;
    asm volatile(
        "ds_read_b128 v[88:91], v82\n\t"
        "v_mov_b32 v83, 0xdddd\n\tv_mov_b32 v84, 0\n\tv_mov_b32 v85, 0\n\tv_mov_b32 v86, 0\n\tv_mov_b32 v87, 0\n\t"
        "s_mov_b32 %0, 4\n\t"
        "s_waitcnt vmcnt(23) lgkmcnt(0)\n\t"
        A_QK(128, 0, 88)
        "s_nop 11\n\t"
        A_BLOCK_W(96, 112, 88, 92, 1024, "v81", "v79", VMW(20), VMW(17), VMW(14), VMW(11), VMW(8), VMW(5), VMW(2), VMW(0))
        "s_branch 2f\n"
        "1:\n\t"
        A_BLOCK(96, 112, 88, 92, 1024, "v81", "v79")
        "2:\n\t"
        A_BLOCK(112, 96, 92, 88, 2048, "v80", "v78")
        "v_add_u32 v82, 0x800, v82\n\t"
        "v_add_u32 v81, 0x2400, v80\n\tv_add_u32 v79, 0x2400, v78\n\t"
        "v_add_u32 v80, 0x4800, v80\n\tv_add_u32 v78, 0x4800, v78\n\t"
        "s_sub_u32 %0, %0, 1\n\ts_cmp_lg_u32 %0, 0\n\ts_cbranch_scc1 1b\n\t"
        A_PV(112, 60, 144, 8)
        "s_nop 15\n\t"
        A_PUT(112, "v81", "v79")
        "s_waitcnt lgkmcnt(0)"
        : "=&s"(cnt), "+{v82}"(qaddr), "+{v81}"(pa16), "+{v80}"(p16), "+{v79}"(pa4), "+{v78}"(p4)
        : "{v[160:175]}"(kv.k0), "{v[176:191]}"(kv.k1), "{v[192:207]}"(kv.v0), "{v[208:223]}"(kv.v1), "{v[224:239]}"(kv.v2), "{v[240:255]}"(kv.v3)
        : "memory", "scc", CL8(83, 84, 85, 86, 87, 88, 89, 90), CL8(91, 92, 93, 94, 95, 96, 97, 98), CL8(99, 100, 101, 102, 103, 104, 105, 106),
          CL8(107, 108, 109, 110, 111, 112, 113, 114), CL8(115, 116, 117, 118, 119, 120, 121, 122), CL8(123, 124, 125, 126, 127, 128, 129, 130),
          CL8(131, 132, 133, 134, 135, 136, 137, 138), CL8(139, 140, 141, 142, 143, 144, 145, 146), CL8(147, 148, 149, 150, 151, 152, 153, 154),
          "v155", "v156", "v157", "v158", "v159");
}
static_assert(4 * P_SZ == 0x2400, "partial stride");
DEVI u32x4 asu(bf16x8 v) { return __builtin_bit_cast(u32x4, v); }

struct PosTab { unsigned short v[4096];
    constexpr PosTab() : v{} {
        for (int y = 0; y < 256; ++y) for (int i = 0; i < 16; ++i) { const int l = 2047 - 8 * y - i; int p = l;
            if (l < 0) p = 2049;
            else if (l >= 256) { int sh = 0; while ((l >> sh) >= 256) ++sh; int q = l >> sh; const int r = l & ((1 << sh) - 1), half = 1 << (sh - 1);
                if (r > half || (r == half && (q & 1))) ++q; p = q << sh; }
            v[y * 16 + i] = (unsigned short)(2 * p); } } };
__device__ const PosTab pos_tab = PosTab();

#define CONV_ASM_0 \
    "ds_read_b64 v[78:79], %4 offset:4032\n\t" \
    "ds_read_b64 v[80:81], %4 offset:4040\n\t" \
    "ds_read_b128 v[138:141], %5 offset:4032\n\t" \
    "ds_read_b128 v[126:129], %5 offset:2496\n\t" \
    "ds_read_b128 v[122:125], %5 offset:1984\n\t" \
    "ds_read_b128 v[110:113], %5 offset:448\n\t" \
    "ds_read_b64 v[82:83], %4 offset:3520\n\t" \
    "ds_read_b64 v[84:85], %4 offset:3528\n\t" \
    "ds_read_b128 v[134:137], %5 offset:3520\n\t" \
    "ds_read_b128 v[118:121], %5 offset:1472\n\t" \
    "ds_read_b64 v[86:87], %4 offset:3008\n\t" \
    "ds_read_b64 v[88:89], %4 offset:3016\n\t" \
    "ds_read_b128 v[130:133], %5 offset:3008\n\t" \
    "ds_read_b128 v[114:117], %5 offset:960\n\t" \
    "ds_read_b64 v[90:91], %4 offset:2496\n\t" \
    "ds_read_b64 v[92:93], %4 offset:2504\n\t" \
    "ds_read_b64 v[94:95], %4 offset:1984\n\t" \
    "ds_read_b64 v[96:97], %4 offset:1992\n\t" \
    "ds_read_b64 v[98:99], %4 offset:1472\n\t" \
    "ds_read_b64 v[100:101], %4 offset:1480\n\t" \
    "ds_read_b64 v[102:103], %4 offset:960\n\t" \
    "ds_read_b64 v[104:105], %4 offset:968\n\t" \
    "ds_read_b64 v[106:107], %4 offset:448\n\t" \
    "ds_read_b64 v[108:109], %4 offset:456\n\t" \
    "s_waitcnt lgkmcnt(15)\n\t" \
    "v_mfma_f32_16x16x32_bf16 v[154:157], v[78:81], v[138:141], 0\n\t" \
    "v_mfma_f32_16x16x32_bf16 v[150:153], v[78:81], v[126:129], 0\n\t" \
    "v_mfma_f32_16x16x32_bf16 v[146:149], v[78:81], v[122:125], 0\n\t" \
    "v_mfma_f32_16x16x32_bf16 v[142:145], v[78:81], v[110:113], 0\n\t" \
    "ds_read_b64 v[78:79], %4 offset:3968\n\t" \
    "ds_read_b64 v[80:81], %4 offset:3976\n\t" \
    "ds_read_b128 v[138:141], %5 offset:3968\n\t" \
    "v_mfma_f32_16x16x32_bf16 v[154:157], v[82:85], v[134:137], v[154:157]\n\t" \
    "v_mfma_f32_16x16x32_bf16 v[150:153], v[82:85], v[122:125], v[150:153]\n\t" \
    "s_waitcnt lgkmcnt(15)\n\t" \
    "v_mfma_f32_16x16x32_bf16 v[146:149], v[82:85], v[118:121], v[146:149]\n\t" \
    "ds_read_b64 v[82:83], %4 offset:3456\n\t" \
    "ds_read_b64 v[84:85], %4 offset:3464\n\t" \
    "ds_read_b128 v[134:137], %5 offset:3456\n\t" \
    "s_waitcnt lgkmcnt(15)\n\t" \
    "v_mfma_f32_16x16x32_bf16 v[154:157], v[86:89], v[130:133], v[154:157]\n\t" \
    "v_mfma_f32_16x16x32_bf16 v[150:153], v[86:89], v[118:121], v[150:153]\n\t" \
    "v_mfma_f32_16x16x32_bf16 v[146:149], v[86:89], v[114:117], v[146:149]\n\t" \
    "ds_read_b64 v[86:87], %4 offset:2944\n\t" \
    "ds_read_b64 v[88:89], %4 offset:2952\n\t" \
    "ds_read_b128 v[130:133], %5 offset:2944\n\t" \
    "s_waitcnt lgkmcnt(15)\n\t" \
    "v_mfma_f32_16x16x32_bf16 v[154:157], v[90:93], v[126:129], v[154:157]\n\t" \
    "v_mfma_f32_16x16x32_bf16 v[150:153], v[90:93], v[114:117], v[150:153]\n\t" \
    "v_mfma_f32_16x16x32_bf16 v[146:149], v[90:93], v[110:113], v[146:149]\n\t" \
    "ds_read_b64 v[90:91], %4 offset:2432\n\t" \
    "ds_read_b64 v[92:93], %4 offset:2440\n\t" \
    "ds_read_b128 v[126:129], %5 offset:2432\n\t" \
    "v_mfma_f32_16x16x32_bf16 v[154:157], v[94:97], v[122:125], v[154:157]\n\t" \
    "v_mfma_f32_16x16x32_bf16 v[150:153], v[94:97], v[110:113], v[150:153]\n\t" \
    "ds_read_b64 v[94:95], %4 offset:1920\n\t" \
    "ds_read_b64 v[96:97], %4 offset:1928\n\t" \
    "ds_read_b128 v[122:125], %5 offset:1920\n\t" \
    "s_waitcnt lgkmcnt(15)\n\t" \
    "v_mfma_f32_16x16x32_bf16 v[154:157], v[98:101], v[118:121], v[154:157]\n\t" \
    "ds_read_b64 v[98:99], %4 offset:1408\n\t" \
    "ds_read_b64 v[100:101], %4 offset:1416\n\t" \
    "ds_read_b128 v[118:121], %5 offset:1408\n\t" \
    "v_mfma_f32_16x16x32_bf16 v[154:157], v[102:105], v[114:117], v[154:157]\n\t" \
    "ds_read_b64 v[102:103], %4 offset:896\n\t" \
    "ds_read_b64 v[104:105], %4 offset:904\n\t" \
    "ds_read_b128 v[114:117], %5 offset:896\n\t" \
    "v_mfma_f32_16x16x32_bf16 v[154:157], v[106:109], v[110:113], v[154:157]\n\t" \
    "ds_read_b64 v[106:107], %4 offset:384\n\t" \
    "ds_read_b64 v[108:109], %4 offset:392\n\t" \
    "ds_read_b128 v[110:113], %5 offset:384\n\t" \
    "s_waitcnt lgkmcnt(15)\n\t" \
    "v_mfma_f32_16x16x32_bf16 v[154:157], v[78:81], v[138:141], v[154:157]\n\t" \
    "s_waitcnt lgkmcnt(12)\n\t" \
    "v_mfma_f32_16x16x32_bf16 v[150:153], v[78:81], v[126:129], v[150:153]\n\t" \
    "s_waitcnt lgkmcnt(9)\n\t" \
    "v_mfma_f32_16x16x32_bf16 v[146:149], v[78:81], v[122:125], v[146:149]\n\t" \
    "ds_read_b128 v[138:141], %5 offset:3904\n\t" \
    "v_mfma_f32_16x16x32_bf16 v[154:157], v[82:85], v[134:137], v[154:157]\n\t" \
    "v_mfma_f32_16x16x32_bf16 v[150:153], v[82:85], v[122:125], v[150:153]\n\t" \
    "s_waitcnt lgkmcnt(7)\n\t" \
    "v_mfma_f32_16x16x32_bf16 v[146:149], v[82:85], v[118:121], v[146:149]\n\t" \
    "ds_read_b64 v[82:83], %4 offset:3392\n\t" \
    "ds_read_b64 v[84:85], %4 offset:3400\n\t" \
    "ds_read_b128 v[134:137], %5 offset:3392\n\t" \
    "v_mfma_f32_16x16x32_bf16 v[154:157], v[86:89], v[130:133], v[154:157]\n\t" \
    "v_mfma_f32_16x16x32_bf16 v[150:153], v[86:89], v[118:121], v[150:153]\n\t" \
    "s_waitcnt lgkmcnt(7)\n\t" \
    "v_mfma_f32_16x16x32_bf16 v[146:149], v[86:89], v[114:117], v[146:149]\n\t" \
    "s_waitcnt lgkmcnt(4)\n\t" \
    "v_mfma_f32_16x16x32_bf16 v[142:145], v[78:81], v[110:113], v[142:145]\n\t" \
    "ds_read_b64 v[78:79], %4 offset:3904\n\t" \
    "ds_read_b64 v[80:81], %4 offset:3912\n\t" \
    "ds_read_b64 v[86:87], %4 offset:2880\n\t" \
    "ds_read_b64 v[88:89], %4 offset:2888\n\t" \
    "ds_read_b128 v[130:133], %5 offset:2880\n\t" \
    "v_mfma_f32_16x16x32_bf16 v[154:157], v[90:93], v[126:129], v[154:157]\n\t" \
    "v_mfma_f32_16x16x32_bf16 v[150:153], v[90:93], v[114:117], v[150:153]\n\t" \
    "v_mfma_f32_16x16x32_bf16 v[146:149], v[90:93], v[110:113], v[146:149]\n\t" \
    "ds_read_b64 v[90:91], %4 offset:2368\n\t" \
    "ds_read_b64 v[92:93], %4 offset:2376\n\t" \
    "ds_read_b128 v[126:129], %5 offset:2368\n\t" \
    "v_mfma_f32_16x16x32_bf16 v[154:157], v[94:97], v[122:125], v[154:157]\n\t" \
    "v_mfma_f32_16x16x32_bf16 v[150:153], v[94:97], v[110:113], v[150:153]\n\t" \
    "ds_read_b64 v[94:95], %4 offset:1856\n\t" \
    "ds_read_b64 v[96:97], %4 offset:1864\n\t" \
    "ds_read_b128 v[122:125], %5 offset:1856\n\t" \
    "v_mfma_f32_16x16x32_bf16 v[154:157], v[98:101], v[118:121], v[154:157]\n\t" \
    "ds_read_b64 v[98:99], %4 offset:1344\n\t" \
    "ds_read_b64 v[100:101], %4 offset:1352\n\t" \
    "ds_read_b128 v[118:121], %5 offset:1344\n\t" \
    "v_mfma_f32_16x16x32_bf16 v[154:157], v[102:105], v[114:117], v[154:157]\n\t" \
    "ds_read_b64 v[102:103], %4 offset:832\n\t" \
    "ds_read_b64 v[104:105], %4 offset:840\n\t" \
    "ds_read_b128 v[114:117], %5 offset:832\n\t" \
    "v_mfma_f32_16x16x32_bf16 v[154:157], v[106:109], v[110:113], v[154:157]\n\t" \
    "ds_read_b64 v[106:107], %4 offset:320\n\t" \
    "ds_read_b64 v[108:109], %4 offset:328\n\t" \
    "ds_read_b128 v[110:113], %5 offset:320\n\t" \
    "s_waitcnt lgkmcnt(15)\n\t" \
    "v_mfma_f32_16x16x32_bf16 v[154:157], v[78:81], v[138:141], v[154:157]\n\t" \
    "s_waitcnt lgkmcnt(12)\n\t" \
    "v_mfma_f32_16x16x32_bf16 v[150:153], v[78:81], v[126:129], v[150:153]\n\t" \
    "s_waitcnt lgkmcnt(9)\n\t" \
    "v_mfma_f32_16x16x32_bf16 v[146:149], v[78:81], v[122:125], v[146:149]\n\t" \
    "ds_read_b128 v[138:141], %5 offset:3840\n\t" \
    "v_mfma_f32_16x16x32_bf16 v[154:157], v[82:85], v[134:137], v[154:157]\n\t" \
    "v_mfma_f32_16x16x32_bf16 v[150:153], v[82:85], v[122:125], v[150:153]\n\t" \
    "s_waitcnt lgkmcnt(7)\n\t" \
    "v_mfma_f32_16x16x32_bf16 v[146:149], v[82:85], v[118:121], v[146:149]\n\t" \
    "ds_read_b64 v[82:83], %4 offset:3328\n\t" \
    "ds_read_b64 v[84:85], %4 offset:3336\n\t" \
    "ds_read_b128 v[134:137], %5 offset:3328\n\t" \
    "v_mfma_f32_16x16x32_bf16 v[154:157], v[86:89], v[130:133], v[154:157]\n\t" \
    "v_mfma_f32_16x16x32_bf16 v[150:153], v[86:89], v[118:121], v[150:153]\n\t" \
    "s_waitcnt lgkmcnt(7)\n\t" \
    "v_mfma_f32_16x16x32_bf16 v[146:149], v[86:89], v[114:117], v[146:149]\n\t" \
    "s_waitcnt lgkmcnt(4)\n\t" \
    "v_mfma_f32_16x16x32_bf16 v[142:145], v[78:81], v[110:113], v[142:145]\n\t" \
    "ds_read_b64 v[78:79], %4 offset:3840\n\t" \
    "ds_read_b64 v[80:81], %4 offset:3848\n\t" \
    "ds_read_b64 v[86:87], %4 offset:2816\n\t" \
    "ds_read_b64 v[88:89], %4 offset:2824\n\t" \
    "ds_read_b128 v[130:133], %5 offset:2816\n\t" \
    "v_mfma_f32_16x16x32_bf16 v[154:157], v[90:93], v[126:129], v[154:157]\n\t" \
    "v_mfma_f32_16x16x32_bf16 v[150:153], v[90:93], v[114:117], v[150:153]\n\t" \
    "v_mfma_f32_16x16x32_bf16 v[146:149], v[90:93], v[110:113], v[146:149]\n\t" \
    "ds_read_b64 v[90:91], %4 offset:2304\n\t" \
    "ds_read_b64 v[92:93], %4 offset:2312\n\t" \
    "ds_read_b128 v[126:129], %5 offset:2304\n\t" \
    "v_mfma_f32_16x16x32_bf16 v[154:157], v[94:97], v[122:125], v[154:157]\n\t" \
    "v_mfma_f32_16x16x32_bf16 v[150:153], v[94:97], v[110:113], v[150:153]\n\t" \
    "ds_read_b64 v[94:95], %4 offset:1792\n\t" \
    "ds_read_b64 v[96:97], %4 offset:1800\n\t" \
    "ds_read_b128 v[122:125], %5 offset:1792\n\t" \
    "v_mfma_f32_16x16x32_bf16 v[154:157], v[98:101], v[118:121], v[154:157]\n\t" \
    "ds_read_b64 v[98:99], %4 offset:1280\n\t" \
    "ds_read_b64 v[100:101], %4 offset:1288\n\t" \
    "ds_read_b128 v[118:121], %5 offset:1280\n\t" \
    "v_mfma_f32_16x16x32_bf16 v[154:157], v[102:105], v[114:117], v[154:157]\n\t" \
    "ds_read_b64 v[102:103], %4 offset:768\n\t" \
    "ds_read_b64 v[104:105], %4 offset:776\n\t" \
    "ds_read_b128 v[114:117], %5 offset:768\n\t" \
    "v_mfma_f32_16x16x32_bf16 v[154:157], v[106:109], v[110:113], v[154:157]\n\t" \
    "ds_read_b64 v[106:107], %4 offset:256\n\t" \
    "ds_read_b64 v[108:109], %4 offset:264\n\t" \
    "ds_read_b128 v[110:113], %5 offset:256\n\t" \
    "s_waitcnt lgkmcnt(15)\n\t" \
    "v_mfma_f32_16x16x32_bf16 v[154:157], v[78:81], v[138:141], v[154:157]\n\t" \
    "s_waitcnt lgkmcnt(12)\n\t" \
    "v_mfma_f32_16x16x32_bf16 v[150:153], v[78:81], v[126:129], v[150:153]\n\t" \
    "s_waitcnt lgkmcnt(9)\n\t" \
    "v_mfma_f32_16x16x32_bf16 v[146:149], v[78:81], v[122:125], v[146:149]\n\t" \
    "ds_read_b128 v[138:141], %5 offset:3776\n\t" \
    "v_mfma_f32_16x16x32_bf16 v[154:157], v[82:85], v[134:137], v[154:157]\n\t" \
    "v_mfma_f32_16x16x32_bf16 v[150:153], v[82:85], v[122:125], v[150:153]\n\t" \
    "s_waitcnt lgkmcnt(7)\n\t" \
    "v_mfma_f32_16x16x32_bf16 v[146:149], v[82:85], v[118:121], v[146:149]\n\t" \
    "ds_read_b64 v[82:83], %4 offset:3264\n\t" \
    "ds_read_b64 v[84:85], %4 offset:3272\n\t" \
    "ds_read_b128 v[134:137], %5 offset:3264\n\t" \
    "v_mfma_f32_16x16x32_bf16 v[154:157], v[86:89], v[130:133], v[154:157]\n\t" \
    "v_mfma_f32_16x16x32_bf16 v[150:153], v[86:89], v[118:121], v[150:153]\n\t" \
    "s_waitcnt lgkmcnt(7)\n\t" \
    "v_mfma_f32_16x16x32_bf16 v[146:149], v[86:89], v[114:117], v[146:149]\n\t" \
    "s_waitcnt lgkmcnt(4)\n\t" \
    "v_mfma_f32_16x16x32_bf16 v[142:145], v[78:81], v[110:113], v[142:145]\n\t" \
    "ds_read_b64 v[78:79], %4 offset:3776\n\t" \
    "ds_read_b64 v[80:81], %4 offset:3784\n\t" \
    "ds_read_b64 v[86:87], %4 offset:2752\n\t" \
    "ds_read_b64 v[88:89], %4 offset:2760\n\t" \
    "ds_read_b128 v[130:133], %5 offset:2752\n\t" \
    "v_mfma_f32_16x16x32_bf16 v[154:157], v[90:93], v[126:129], v[154:157]\n\t" \
    "v_mfma_f32_16x16x32_bf16 v[150:153], v[90:93], v[114:117], v[150:153]\n\t" \
    "v_mfma_f32_16x16x32_bf16 v[146:149], v[90:93], v[110:113], v[146:149]\n\t" \
    "ds_read_b64 v[90:91], %4 offset:2240\n\t" \
    "ds_read_b64 v[92:93], %4 offset:2248\n\t" \
    "ds_read_b128 v[126:129], %5 offset:2240\n\t" \
    "v_mfma_f32_16x16x32_bf16 v[154:157], v[94:97], v[122:125], v[154:157]\n\t" \
    "v_mfma_f32_16x16x32_bf16 v[150:153], v[94:97], v[110:113], v[150:153]\n\t" \
    "ds_read_b64 v[94:95], %4 offset:1728\n\t" \
    "ds_read_b64 v[96:97], %4 offset:1736\n\t" \
    "ds_read_b128 v[122:125], %5 offset:1728\n\t" \
    "v_mfma_f32_16x16x32_bf16 v[154:157], v[98:101], v[118:121], v[154:157]\n\t" \
    "ds_read_b64 v[98:99], %4 offset:1216\n\t" \
    "ds_read_b64 v[100:101], %4 offset:1224\n\t" \
    "ds_read_b128 v[118:121], %5 offset:1216\n\t" \
    "v_mfma_f32_16x16x32_bf16 v[154:157], v[102:105], v[114:117], v[154:157]\n\t" \
    "ds_read_b64 v[102:103], %4 offset:704\n\t" \
    "ds_read_b64 v[104:105], %4 offset:712\n\t" \
    "ds_read_b128 v[114:117], %5 offset:704\n\t" \
    "v_mfma_f32_16x16x32_bf16 v[154:157], v[106:109], v[110:113], v[154:157]\n\t" \
    "ds_read_b64 v[106:107], %4 offset:192\n\t" \
    "ds_read_b64 v[108:109], %4 offset:200\n\t" \
    "ds_read_b128 v[110:113], %5 offset:192\n\t" \
    "s_waitcnt lgkmcnt(15)\n\t" \
    "v_mfma_f32_16x16x32_bf16 v[154:157], v[78:81], v[138:141], v[154:157]\n\t" \
    "s_waitcnt lgkmcnt(12)\n\t" \
    "v_mfma_f32_16x16x32_bf16 v[150:153], v[78:81], v[126:129], v[150:153]\n\t" \
    "s_waitcnt lgkmcnt(9)\n\t" \
    "v_mfma_f32_16x16x32_bf16 v[146:149], v[78:81], v[122:125], v[146:149]\n\t" \
    "ds_read_b128 v[138:141], %5 offset:3712\n\t" \
    "v_mfma_f32_16x16x32_bf16 v[154:157], v[82:85], v[134:137], v[154:157]\n\t" \
    "v_mfma_f32_16x16x32_bf16 v[150:153], v[82:85], v[122:125], v[150:153]\n\t" \
    "s_waitcnt lgkmcnt(7)\n\t" \
    "v_mfma_f32_16x16x32_bf16 v[146:149], v[82:85], v[118:121], v[146:149]\n\t" \
    "ds_read_b64 v[82:83], %4 offset:3200\n\t" \
    "ds_read_b64 v[84:85], %4 offset:3208\n\t" \
    "ds_read_b128 v[134:137], %5 offset:3200\n\t" \
    "v_mfma_f32_16x16x32_bf16 v[154:157], v[86:89], v[130:133], v[154:157]\n\t" \
    "v_mfma_f32_16x16x32_bf16 v[150:153], v[86:89], v[118:121], v[150:153]\n\t" \
    "s_waitcnt lgkmcnt(7)\n\t" \
    "v_mfma_f32_16x16x32_bf16 v[146:149], v[86:89], v[114:117], v[146:149]\n\t" \
    "s_waitcnt lgkmcnt(4)\n\t" \
    "v_mfma_f32_16x16x32_bf16 v[142:145], v[78:81], v[110:113], v[142:145]\n\t" \
    "ds_read_b64 v[78:79], %4 offset:3712\n\t" \
    "ds_read_b64 v[80:81], %4 offset:3720\n\t" \
    "ds_read_b64 v[86:87], %4 offset:2688\n\t" \
    "ds_read_b64 v[88:89], %4 offset:2696\n\t" \
    "ds_read_b128 v[130:133], %5 offset:2688\n\t" \
    "v_mfma_f32_16x16x32_bf16 v[154:157], v[90:93], v[126:129], v[154:157]\n\t" \
    "v_mfma_f32_16x16x32_bf16 v[150:153], v[90:93], v[114:117], v[150:153]\n\t" \
    "v_mfma_f32_16x16x32_bf16 v[146:149], v[90:93], v[110:113], v[146:149]\n\t" \
    "ds_read_b64 v[90:91], %4 offset:2176\n\t" \
    "ds_read_b64 v[92:93], %4 offset:2184\n\t" \
    "ds_read_b128 v[126:129], %5 offset:2176\n\t" \
    "v_mfma_f32_16x16x32_bf16 v[154:157], v[94:97], v[122:125], v[154:157]\n\t" \
    "v_mfma_f32_16x16x32_bf16 v[150:153], v[94:97], v[110:113], v[150:153]\n\t" \
    "ds_read_b64 v[94:95], %4 offset:1664\n\t" \
    "ds_read_b64 v[96:97], %4 offset:1672\n\t" \
    "ds_read_b128 v[122:125], %5 offset:1664\n\t" \
    "v_mfma_f32_16x16x32_bf16 v[154:157], v[98:101], v[118:121], v[154:157]\n\t" \
    "ds_read_b64 v[98:99], %4 offset:1152\n\t" \
    "ds_read_b64 v[100:101], %4 offset:1160\n\t" \
    "ds_read_b128 v[118:121], %5 offset:1152\n\t" \
    "v_mfma_f32_16x16x32_bf16 v[154:157], v[102:105], v[114:117], v[154:157]\n\t" \
    "ds_read_b64 v[102:103], %4 offset:640\n\t" \
    "ds_read_b64 v[104:105], %4 offset:648\n\t" \
    "ds_read_b128 v[114:117], %5 offset:640\n\t" \
    "v_mfma_f32_16x16x32_bf16 v[154:157], v[106:109], v[110:113], v[154:157]\n\t" \
    "ds_read_b64 v[106:107], %4 offset:128\n\t" \
    "ds_read_b64 v[108:109], %4 offset:136\n\t" \
    "ds_read_b128 v[110:113], %5 offset:128\n\t" \
    "s_waitcnt lgkmcnt(15)\n\t" \
    "v_mfma_f32_16x16x32_bf16 v[154:157], v[78:81], v[138:141], v[154:157]\n\t" \
    "s_waitcnt lgkmcnt(12)\n\t" \
    "v_mfma_f32_16x16x32_bf16 v[150:153], v[78:81], v[126:129], v[150:153]\n\t" \
    "s_waitcnt lgkmcnt(9)\n\t" \
    "v_mfma_f32_16x16x32_bf16 v[146:149], v[78:81], v[122:125], v[146:149]\n\t" \
    "ds_read_b128 v[138:141], %5 offset:3648\n\t" \
    "v_mfma_f32_16x16x32_bf16 v[154:157], v[82:85], v[134:137], v[154:157]\n\t" \
    "v_mfma_f32_16x16x32_bf16 v[150:153], v[82:85], v[122:125], v[150:153]\n\t" \
    "s_waitcnt lgkmcnt(7)\n\t" \
    "v_mfma_f32_16x16x32_bf16 v[146:149], v[82:85], v[118:121], v[146:149]\n\t" \
    "ds_read_b64 v[82:83], %4 offset:3136\n\t" \
    "ds_read_b64 v[84:85], %4 offset:3144\n\t" \
    "ds_read_b128 v[134:137], %5 offset:3136\n\t" \
    "v_mfma_f32_16x16x32_bf16 v[154:157], v[86:89], v[130:133], v[154:157]\n\t" \
    "v_mfma_f32_16x16x32_bf16 v[150:153], v[86:89], v[118:121], v[150:153]\n\t" \
    "s_waitcnt lgkmcnt(7)\n\t" \
    "v_mfma_f32_16x16x32_bf16 v[146:149], v[86:89], v[114:117], v[146:149]\n\t" \
    "s_waitcnt lgkmcnt(4)\n\t" \
    "v_mfma_f32_16x16x32_bf16 v[142:145], v[78:81], v[110:113], v[142:145]\n\t" \
    "ds_read_b64 v[78:79], %4 offset:3648\n\t" \
    "ds_read_b64 v[80:81], %4 offset:3656\n\t" \
    "ds_read_b64 v[86:87], %4 offset:2624\n\t" \
    "ds_read_b64 v[88:89], %4 offset:2632\n\t" \
    "ds_read_b128 v[130:133], %5 offset:2624\n\t" \
    "v_mfma_f32_16x16x32_bf16 v[154:157], v[90:93], v[126:129], v[154:157]\n\t" \
    "v_mfma_f32_16x16x32_bf16 v[150:153], v[90:93], v[114:117], v[150:153]\n\t" \
    "v_mfma_f32_16x16x32_bf16 v[146:149], v[90:93], v[110:113], v[146:149]\n\t" \
    "ds_read_b64 v[90:91], %4 offset:2112\n\t" \
    "ds_read_b64 v[92:93], %4 offset:2120\n\t" \
    "ds_read_b128 v[126:129], %5 offset:2112\n\t" \
    "v_mfma_f32_16x16x32_bf16 v[154:157], v[94:97], v[122:125], v[154:157]\n\t" \
    "v_mfma_f32_16x16x32_bf16 v[150:153], v[94:97], v[110:113], v[150:153]\n\t" \
    "ds_read_b64 v[94:95], %4 offset:1600\n\t" \
    "ds_read_b64 v[96:97], %4 offset:1608\n\t" \
    "ds_read_b128 v[122:125], %5 offset:1600\n\t" \
    "v_mfma_f32_16x16x32_bf16 v[154:157], v[98:101], v[118:121], v[154:157]\n\t" \
    "ds_read_b64 v[98:99], %4 offset:1088\n\t" \
    "ds_read_b64 v[100:101], %4 offset:1096\n\t" \
    "ds_read_b128 v[118:121], %5 offset:1088\n\t" \
    "v_mfma_f32_16x16x32_bf16 v[154:157], v[102:105], v[114:117], v[154:157]\n\t" \
    "ds_read_b64 v[102:103], %4 offset:576\n\t" \
    "ds_read_b64 v[104:105], %4 offset:584\n\t" \
    "ds_read_b128 v[114:117], %5 offset:576\n\t" \
    "v_mfma_f32_16x16x32_bf16 v[154:157], v[106:109], v[110:113], v[154:157]\n\t" \
    "ds_read_b64 v[106:107], %4 offset:64\n\t" \
    "ds_read_b64 v[108:109], %4 offset:72\n\t" \
    "ds_read_b128 v[110:113], %5 offset:64\n\t" \
    "s_waitcnt lgkmcnt(15)\n\t" \
    "v_mfma_f32_16x16x32_bf16 v[154:157], v[78:81], v[138:141], v[154:157]\n\t" \
    "s_waitcnt lgkmcnt(12)\n\t" \
    "v_mfma_f32_16x16x32_bf16 v[150:153], v[78:81], v[126:129], v[150:153]\n\t" \
    "s_waitcnt lgkmcnt(9)\n\t" \
    "v_mfma_f32_16x16x32_bf16 v[146:149], v[78:81], v[122:125], v[146:149]\n\t" \
    "ds_read_b128 v[138:141], %5 offset:3584\n\t" \
    "v_mfma_f32_16x16x32_bf16 v[154:157], v[82:85], v[134:137], v[154:157]\n\t" \
    "v_mfma_f32_16x16x32_bf16 v[150:153], v[82:85], v[122:125], v[150:153]\n\t" \
    "s_waitcnt lgkmcnt(7)\n\t" \
    "v_mfma_f32_16x16x32_bf16 v[146:149], v[82:85], v[118:121], v[146:149]\n\t" \
    "ds_read_b64 v[82:83], %4 offset:3072\n\t" \
    "ds_read_b64 v[84:85], %4 offset:3080\n\t" \
    "ds_read_b128 v[134:137], %5 offset:3072\n\t" \
    "v_mfma_f32_16x16x32_bf16 v[154:157], v[86:89], v[130:133], v[154:157]\n\t" \
    "v_mfma_f32_16x16x32_bf16 v[150:153], v[86:89], v[118:121], v[150:153]\n\t" \
    "s_waitcnt lgkmcnt(7)\n\t" \
    "v_mfma_f32_16x16x32_bf16 v[146:149], v[86:89], v[114:117], v[146:149]\n\t" \
    "s_waitcnt lgkmcnt(4)\n\t" \
    "v_mfma_f32_16x16x32_bf16 v[142:145], v[78:81], v[110:113], v[142:145]\n\t" \
    "ds_read_b64 v[78:79], %4 offset:3584\n\t" \
    "ds_read_b64 v[80:81], %4 offset:3592\n\t" \
    "ds_read_b64 v[86:87], %4 offset:2560\n\t" \
    "ds_read_b64 v[88:89], %4 offset:2568\n\t" \
    "ds_read_b128 v[130:133], %5 offset:2560\n\t" \
    "v_mfma_f32_16x16x32_bf16 v[154:157], v[90:93], v[126:129], v[154:157]\n\t" \
    "v_mfma_f32_16x16x32_bf16 v[150:153], v[90:93], v[114:117], v[150:153]\n\t" \
    "v_mfma_f32_16x16x32_bf16 v[146:149], v[90:93], v[110:113], v[146:149]\n\t" \
    "ds_read_b64 v[90:91], %4 offset:2048\n\t" \
    "ds_read_b64 v[92:93], %4 offset:2056\n\t" \
    "ds_read_b128 v[126:129], %5 offset:2048\n\t" \
    "v_mfma_f32_16x16x32_bf16 v[154:157], v[94:97], v[122:125], v[154:157]\n\t" \
    "v_mfma_f32_16x16x32_bf16 v[150:153], v[94:97], v[110:113], v[150:153]\n\t" \
    "ds_read_b64 v[94:95], %4 offset:1536\n\t" \
    "ds_read_b64 v[96:97], %4 offset:1544\n\t" \
    "ds_read_b128 v[122:125], %5 offset:1536\n\t" \
    "v_mfma_f32_16x16x32_bf16 v[154:157], v[98:101], v[118:121], v[154:157]\n\t" \
    "ds_read_b64 v[98:99], %4 offset:1024\n\t" \
    "ds_read_b64 v[100:101], %4 offset:1032\n\t" \
    "ds_read_b128 v[118:121], %5 offset:1024\n\t" \
    "v_mfma_f32_16x16x32_bf16 v[154:157], v[102:105], v[114:117], v[154:157]\n\t" \
    "ds_read_b64 v[102:103], %4 offset:512\n\t" \
    "ds_read_b64 v[104:105], %4 offset:520\n\t" \
    "ds_read_b128 v[114:117], %5 offset:512\n\t" \
    "v_mfma_f32_16x16x32_bf16 v[154:157], v[106:109], v[110:113], v[154:157]\n\t" \
    "ds_read_b64 v[106:107], %4 offset:0\n\t" \
    "ds_read_b64 v[108:109], %4 offset:8\n\t" \
    "ds_read_b128 v[110:113], %5 offset:0\n\t" \
    "s_waitcnt lgkmcnt(15)\n\t" \
    "v_mfma_f32_16x16x32_bf16 v[154:157], v[78:81], v[138:141], v[154:157]\n\t" \
    "s_waitcnt lgkmcnt(12)\n\t" \
    "v_mfma_f32_16x16x32_bf16 v[150:153], v[78:81], v[126:129], v[150:153]\n\t" \
    "s_waitcnt lgkmcnt(9)\n\t" \
    "v_mfma_f32_16x16x32_bf16 v[146:149], v[78:81], v[122:125], v[146:149]\n\t" \
    "v_mfma_f32_16x16x32_bf16 v[154:157], v[82:85], v[134:137], v[154:157]\n\t" \
    "v_mfma_f32_16x16x32_bf16 v[150:153], v[82:85], v[122:125], v[150:153]\n\t" \
    "s_waitcnt lgkmcnt(6)\n\t" \
    "v_mfma_f32_16x16x32_bf16 v[146:149], v[82:85], v[118:121], v[146:149]\n\t" \
    "v_mfma_f32_16x16x32_bf16 v[154:157], v[86:89], v[130:133], v[154:157]\n\t" \
    "v_mfma_f32_16x16x32_bf16 v[150:153], v[86:89], v[118:121], v[150:153]\n\t" \
    "s_waitcnt lgkmcnt(3)\n\t" \
    "v_mfma_f32_16x16x32_bf16 v[146:149], v[86:89], v[114:117], v[146:149]\n\t" \
    "s_waitcnt lgkmcnt(0)\n\t" \
    "v_mfma_f32_16x16x32_bf16 v[142:145], v[78:81], v[110:113], v[142:145]\n\t" \
    "v_mfma_f32_16x16x32_bf16 v[154:157], v[90:93], v[126:129], v[154:157]\n\t" \
    "v_mfma_f32_16x16x32_bf16 v[150:153], v[90:93], v[114:117], v[150:153]\n\t" \
    "v_mfma_f32_16x16x32_bf16 v[146:149], v[90:93], v[110:113], v[146:149]\n\t" \
    "v_mfma_f32_16x16x32_bf16 v[154:157], v[94:97], v[122:125], v[154:157]\n\t" \
    "v_mfma_f32_16x16x32_bf16 v[150:153], v[94:97], v[110:113], v[150:153]\n\t" \
    "v_mfma_f32_16x16x32_bf16 v[154:157], v[98:101], v[118:121], v[154:157]\n\t" \
    "v_mfma_f32_16x16x32_bf16 v[154:157], v[102:105], v[114:117], v[154:157]\n\t" \
    "v_mfma_f32_16x16x32_bf16 v[154:157], v[106:109], v[110:113], v[154:157]\n\t"
#define CONV_ASM_1 \
    "ds_read_b64 v[78:79], %4 offset:4032\n\t" \
    "ds_read_b64 v[80:81], %4 offset:4040\n\t" \
    "ds_read_b128 v[134:137], %5 offset:3520\n\t" \
    "ds_read_b128 v[130:133], %5 offset:3008\n\t" \
    "ds_read_b128 v[118:121], %5 offset:1472\n\t" \
    "ds_read_b128 v[114:117], %5 offset:960\n\t" \
    "ds_read_b64 v[82:83], %4 offset:3520\n\t" \
    "ds_read_b64 v[84:85], %4 offset:3528\n\t" \
    "ds_read_b128 v[126:129], %5 offset:2496\n\t" \
    "ds_read_b128 v[110:113], %5 offset:448\n\t" \
    "ds_read_b64 v[86:87], %4 offset:3008\n\t" \
    "ds_read_b64 v[88:89], %4 offset:3016\n\t" \
    "ds_read_b128 v[122:125], %5 offset:1984\n\t" \
    "ds_read_b64 v[90:91], %4 offset:2496\n\t" \
    "ds_read_b64 v[92:93], %4 offset:2504\n\t" \
    "ds_read_b64 v[94:95], %4 offset:1984\n\t" \
    "ds_read_b64 v[96:97], %4 offset:1992\n\t" \
    "ds_read_b64 v[98:99], %4 offset:1472\n\t" \
    "ds_read_b64 v[100:101], %4 offset:1480\n\t" \
    "ds_read_b64 v[102:103], %4 offset:960\n\t" \
    "ds_read_b64 v[104:105], %4 offset:968\n\t" \
    "s_waitcnt lgkmcnt(15)\n\t" \
    "v_mfma_f32_16x16x32_bf16 v[154:157], v[78:81], v[134:137], 0\n\t" \
    "v_mfma_f32_16x16x32_bf16 v[150:153], v[78:81], v[130:133], 0\n\t" \
    "v_mfma_f32_16x16x32_bf16 v[146:149], v[78:81], v[118:121], 0\n\t" \
    "v_mfma_f32_16x16x32_bf16 v[142:145], v[78:81], v[114:117], 0\n\t" \
    "ds_read_b64 v[78:79], %4 offset:3968\n\t" \
    "ds_read_b64 v[80:81], %4 offset:3976\n\t" \
    "ds_read_b128 v[134:137], %5 offset:3456\n\t" \
    "s_waitcnt lgkmcnt(15)\n\t" \
    "v_mfma_f32_16x16x32_bf16 v[154:157], v[82:85], v[130:133], v[154:157]\n\t" \
    "v_mfma_f32_16x16x32_bf16 v[150:153], v[82:85], v[126:129], v[150:153]\n\t" \
    "v_mfma_f32_16x16x32_bf16 v[146:149], v[82:85], v[114:117], v[146:149]\n\t" \
    "s_waitcnt lgkmcnt(14)\n\t" \
    "v_mfma_f32_16x16x32_bf16 v[142:145], v[82:85], v[110:113], v[142:145]\n\t" \
    "ds_read_b64 v[82:83], %4 offset:3456\n\t" \
    "ds_read_b64 v[84:85], %4 offset:3464\n\t" \
    "ds_read_b128 v[130:133], %5 offset:2944\n\t" \
    "s_waitcnt lgkmcnt(15)\n\t" \
    "v_mfma_f32_16x16x32_bf16 v[154:157], v[86:89], v[126:129], v[154:157]\n\t" \
    "s_waitcnt lgkmcnt(14)\n\t" \
    "v_mfma_f32_16x16x32_bf16 v[150:153], v[86:89], v[122:125], v[150:153]\n\t" \
    "v_mfma_f32_16x16x32_bf16 v[146:149], v[86:89], v[110:113], v[146:149]\n\t" \
    "ds_read_b64 v[86:87], %4 offset:2944\n\t" \
    "ds_read_b64 v[88:89], %4 offset:2952\n\t" \
    "ds_read_b128 v[126:129], %5 offset:2432\n\t" \
    "s_waitcnt lgkmcnt(15)\n\t" \
    "v_mfma_f32_16x16x32_bf16 v[154:157], v[90:93], v[122:125], v[154:157]\n\t" \
    "v_mfma_f32_16x16x32_bf16 v[150:153], v[90:93], v[118:121], v[150:153]\n\t" \
    "ds_read_b64 v[90:91], %4 offset:2432\n\t" \
    "ds_read_b64 v[92:93], %4 offset:2440\n\t" \
    "ds_read_b128 v[122:125], %5 offset:1920\n\t" \
    "s_waitcnt lgkmcnt(15)\n\t" \
    "v_mfma_f32_16x16x32_bf16 v[154:157], v[94:97], v[118:121], v[154:157]\n\t" \
    "v_mfma_f32_16x16x32_bf16 v[150:153], v[94:97], v[114:117], v[150:153]\n\t" \
    "ds_read_b64 v[94:95], %4 offset:1920\n\t" \
    "ds_read_b64 v[96:97], %4 offset:1928\n\t" \
    "ds_read_b128 v[118:121], %5 offset:1408\n\t" \
    "s_waitcnt lgkmcnt(15)\n\t" \
    "v_mfma_f32_16x16x32_bf16 v[154:157], v[98:101], v[114:117], v[154:157]\n\t" \
    "v_mfma_f32_16x16x32_bf16 v[150:153], v[98:101], v[110:113], v[150:153]\n\t" \
    "ds_read_b64 v[98:99], %4 offset:1408\n\t" \
    "ds_read_b64 v[100:101], %4 offset:1416\n\t" \
    "ds_read_b128 v[114:117], %5 offset:896\n\t" \
    "v_mfma_f32_16x16x32_bf16 v[154:157], v[102:105], v[110:113], v[154:157]\n\t" \
    "ds_read_b64 v[102:103], %4 offset:896\n\t" \
    "ds_read_b64 v[104:105], %4 offset:904\n\t" \
    "ds_read_b128 v[110:113], %5 offset:384\n\t" \
    "s_waitcnt lgkmcnt(15)\n\t" \
    "v_mfma_f32_16x16x32_bf16 v[154:157], v[78:81], v[134:137], v[154:157]\n\t" \
    "v_mfma_f32_16x16x32_bf16 v[150:153], v[78:81], v[130:133], v[150:153]\n\t" \
    "s_waitcnt lgkmcnt(6)\n\t" \
    "v_mfma_f32_16x16x32_bf16 v[146:149], v[78:81], v[118:121], v[146:149]\n\t" \
    "s_waitcnt lgkmcnt(3)\n\t" \
    "v_mfma_f32_16x16x32_bf16 v[142:145], v[78:81], v[114:117], v[142:145]\n\t" \
    "ds_read_b64 v[78:79], %4 offset:3904\n\t" \
    "ds_read_b64 v[80:81], %4 offset:3912\n\t" \
    "ds_read_b128 v[134:137], %5 offset:3392\n\t" \
    "v_mfma_f32_16x16x32_bf16 v[154:157], v[82:85], v[130:133], v[154:157]\n\t" \
    "v_mfma_f32_16x16x32_bf16 v[150:153], v[82:85], v[126:129], v[150:153]\n\t" \
    "v_mfma_f32_16x16x32_bf16 v[146:149], v[82:85], v[114:117], v[146:149]\n\t" \
    "s_waitcnt lgkmcnt(3)\n\t" \
    "v_mfma_f32_16x16x32_bf16 v[142:145], v[82:85], v[110:113], v[142:145]\n\t" \
    "ds_read_b64 v[82:83], %4 offset:3392\n\t" \
    "ds_read_b64 v[84:85], %4 offset:3400\n\t" \
    "ds_read_b128 v[130:133], %5 offset:2880\n\t" \
    "v_mfma_f32_16x16x32_bf16 v[154:157], v[86:89], v[126:129], v[154:157]\n\t" \
    "v_mfma_f32_16x16x32_bf16 v[150:153], v[86:89], v[122:125], v[150:153]\n\t" \
    "v_mfma_f32_16x16x32_bf16 v[146:149], v[86:89], v[110:113], v[146:149]\n\t" \
    "ds_read_b64 v[86:87], %4 offset:2880\n\t" \
    "ds_read_b64 v[88:89], %4 offset:2888\n\t" \
    "ds_read_b128 v[126:129], %5 offset:2368\n\t" \
    "v_mfma_f32_16x16x32_bf16 v[154:157], v[90:93], v[122:125], v[154:157]\n\t" \
    "v_mfma_f32_16x16x32_bf16 v[150:153], v[90:93], v[118:121], v[150:153]\n\t" \
    "ds_read_b64 v[90:91], %4 offset:2368\n\t" \
    "ds_read_b64 v[92:93], %4 offset:2376\n\t" \
    "ds_read_b128 v[122:125], %5 offset:1856\n\t" \
    "v_mfma_f32_16x16x32_bf16 v[154:157], v[94:97], v[118:121], v[154:157]\n\t" \
    "v_mfma_f32_16x16x32_bf16 v[150:153], v[94:97], v[114:117], v[150:153]\n\t" \
    "ds_read_b64 v[94:95], %4 offset:1856\n\t" \
    "ds_read_b64 v[96:97], %4 offset:1864\n\t" \
    "ds_read_b128 v[118:121], %5 offset:1344\n\t" \
    "v_mfma_f32_16x16x32_bf16 v[154:157], v[98:101], v[114:117], v[154:157]\n\t" \
    "v_mfma_f32_16x16x32_bf16 v[150:153], v[98:101], v[110:113], v[150:153]\n\t" \
    "ds_read_b64 v[98:99], %4 offset:1344\n\t" \
    "ds_read_b64 v[100:101], %4 offset:1352\n\t" \
    "ds_read_b128 v[114:117], %5 offset:832\n\t" \
    "v_mfma_f32_16x16x32_bf16 v[154:157], v[102:105], v[110:113], v[154:157]\n\t" \
    "ds_read_b64 v[102:103], %4 offset:832\n\t" \
    "ds_read_b64 v[104:105], %4 offset:840\n\t" \
    "ds_read_b128 v[110:113], %5 offset:320\n\t" \
    "s_waitcnt lgkmcnt(15)\n\t" \
    "v_mfma_f32_16x16x32_bf16 v[154:157], v[78:81], v[134:137], v[154:157]\n\t" \
    "v_mfma_f32_16x16x32_bf16 v[150:153], v[78:81], v[130:133], v[150:153]\n\t" \
    "s_waitcnt lgkmcnt(6)\n\t" \
    "v_mfma_f32_16x16x32_bf16 v[146:149], v[78:81], v[118:121], v[146:149]\n\t" \
    "s_waitcnt lgkmcnt(3)\n\t" \
    "v_mfma_f32_16x16x32_bf16 v[142:145], v[78:81], v[114:117], v[142:145]\n\t" \
    "ds_read_b64 v[78:79], %4 offset:3840\n\t" \
    "ds_read_b64 v[80:81], %4 offset:3848\n\t" \
    "ds_read_b128 v[134:137], %5 offset:3328\n\t" \
    "v_mfma_f32_16x16x32_bf16 v[154:157], v[82:85], v[130:133], v[154:157]\n\t" \
    "v_mfma_f32_16x16x32_bf16 v[150:153], v[82:85], v[126:129], v[150:153]\n\t" \
    "v_mfma_f32_16x16x32_bf16 v[146:149], v[82:85], v[114:117], v[146:149]\n\t" \
    "s_waitcnt lgkmcnt(3)\n\t" \
    "v_mfma_f32_16x16x32_bf16 v[142:145], v[82:85], v[110:113], v[142:145]\n\t" \
    "ds_read_b64 v[82:83], %4 offset:3328\n\t" \
    "ds_read_b64 v[84:85], %4 offset:3336\n\t" \
    "ds_read_b128 v[130:133], %5 offset:2816\n\t" \
    "v_mfma_f32_16x16x32_bf16 v[154:157], v[86:89], v[126:129], v[154:157]\n\t" \
    "v_mfma_f32_16x16x32_bf16 v[150:153], v[86:89], v[122:125], v[150:153]\n\t" \
    "v_mfma_f32_16x16x32_bf16 v[146:149], v[86:89], v[110:113], v[146:149]\n\t" \
    "ds_read_b64 v[86:87], %4 offset:2816\n\t" \
    "ds_read_b64 v[88:89], %4 offset:2824\n\t" \
    "ds_read_b128 v[126:129], %5 offset:2304\n\t" \
    "v_mfma_f32_16x16x32_bf16 v[154:157], v[90:93], v[122:125], v[154:157]\n\t" \
    "v_mfma_f32_16x16x32_bf16 v[150:153], v[90:93], v[118:121], v[150:153]\n\t" \
    "ds_read_b64 v[90:91], %4 offset:2304\n\t" \
    "ds_read_b64 v[92:93], %4 offset:2312\n\t" \
    "ds_read_b128 v[122:125], %5 offset:1792\n\t" \
    "v_mfma_f32_16x16x32_bf16 v[154:157], v[94:97], v[118:121], v[154:157]\n\t" \
    "v_mfma_f32_16x16x32_bf16 v[150:153], v[94:97], v[114:117], v[150:153]\n\t" \
    "ds_read_b64 v[94:95], %4 offset:1792\n\t" \
    "ds_read_b64 v[96:97], %4 offset:1800\n\t" \
    "ds_read_b128 v[118:121], %5 offset:1280\n\t" \
    "v_mfma_f32_16x16x32_bf16 v[154:157], v[98:101], v[114:117], v[154:157]\n\t" \
    "v_mfma_f32_16x16x32_bf16 v[150:153], v[98:101], v[110:113], v[150:153]\n\t" \
    "ds_read_b64 v[98:99], %4 offset:1280\n\t" \
    "ds_read_b64 v[100:101], %4 offset:1288\n\t" \
    "ds_read_b128 v[114:117], %5 offset:768\n\t" \
    "v_mfma_f32_16x16x32_bf16 v[154:157], v[102:105], v[110:113], v[154:157]\n\t" \
    "ds_read_b64 v[102:103], %4 offset:768\n\t" \
    "ds_read_b64 v[104:105], %4 offset:776\n\t" \
    "ds_read_b128 v[110:113], %5 offset:256\n\t" \
    "s_waitcnt lgkmcnt(15)\n\t" \
    "v_mfma_f32_16x16x32_bf16 v[154:157], v[78:81], v[134:137], v[154:157]\n\t" \
    "v_mfma_f32_16x16x32_bf16 v[150:153], v[78:81], v[130:133], v[150:153]\n\t" \
    "s_waitcnt lgkmcnt(6)\n\t" \
    "v_mfma_f32_16x16x32_bf16 v[146:149], v[78:81], v[118:121], v[146:149]\n\t" \
    "s_waitcnt lgkmcnt(3)\n\t" \
    "v_mfma_f32_16x16x32_bf16 v[142:145], v[78:81], v[114:117], v[142:145]\n\t" \
    "ds_read_b64 v[78:79], %4 offset:3776\n\t" \
    "ds_read_b64 v[80:81], %4 offset:3784\n\t" \
    "ds_read_b128 v[134:137], %5 offset:3264\n\t" \
    "v_mfma_f32_16x16x32_bf16 v[154:157], v[82:85], v[130:133], v[154:157]\n\t" \
    "v_mfma_f32_16x16x32_bf16 v[150:153], v[82:85], v[126:129], v[150:153]\n\t" \
    "v_mfma_f32_16x16x32_bf16 v[146:149], v[82:85], v[114:117], v[146:149]\n\t" \
    "s_waitcnt lgkmcnt(3)\n\t" \
    "v_mfma_f32_16x16x32_bf16 v[142:145], v[82:85], v[110:113], v[142:145]\n\t" \
    "ds_read_b64 v[82:83], %4 offset:3264\n\t" \
    "ds_read_b64 v[84:85], %4 offset:3272\n\t" \
    "ds_read_b128 v[130:133], %5 offset:2752\n\t" \
    "v_mfma_f32_16x16x32_bf16 v[154:157], v[86:89], v[126:129], v[154:157]\n\t" \
    "v_mfma_f32_16x16x32_bf16 v[150:153], v[86:89], v[122:125], v[150:153]\n\t" \
    "v_mfma_f32_16x16x32_bf16 v[146:149], v[86:89], v[110:113], v[146:149]\n\t" \
    "ds_read_b64 v[86:87], %4 offset:2752\n\t" \
    "ds_read_b64 v[88:89], %4 offset:2760\n\t" \
    "ds_read_b128 v[126:129], %5 offset:2240\n\t" \
    "v_mfma_f32_16x16x32_bf16 v[154:157], v[90:93], v[122:125], v[154:157]\n\t" \
    "v_mfma_f32_16x16x32_bf16 v[150:153], v[90:93], v[118:121], v[150:153]\n\t" \
    "ds_read_b64 v[90:91], %4 offset:2240\n\t" \
    "ds_read_b64 v[92:93], %4 offset:2248\n\t" \
    "ds_read_b128 v[122:125], %5 offset:1728\n\t" \
    "v_mfma_f32_16x16x32_bf16 v[154:157], v[94:97], v[118:121], v[154:157]\n\t" \
    "v_mfma_f32_16x16x32_bf16 v[150:153], v[94:97], v[114:117], v[150:153]\n\t" \
    "ds_read_b64 v[94:95], %4 offset:1728\n\t" \
    "ds_read_b64 v[96:97], %4 offset:1736\n\t" \
    "ds_read_b128 v[118:121], %5 offset:1216\n\t" \
    "v_mfma_f32_16x16x32_bf16 v[154:157], v[98:101], v[114:117], v[154:157]\n\t" \
    "v_mfma_f32_16x16x32_bf16 v[150:153], v[98:101], v[110:113], v[150:153]\n\t" \
    "ds_read_b64 v[98:99], %4 offset:1216\n\t" \
    "ds_read_b64 v[100:101], %4 offset:1224\n\t" \
    "ds_read_b128 v[114:117], %5 offset:704\n\t" \
    "v_mfma_f32_16x16x32_bf16 v[154:157], v[102:105], v[110:113], v[154:157]\n\t" \
    "ds_read_b64 v[102:103], %4 offset:704\n\t" \
    "ds_read_b64 v[104:105], %4 offset:712\n\t" \
    "ds_read_b128 v[110:113], %5 offset:192\n\t" \
    "s_waitcnt lgkmcnt(15)\n\t" \
    "v_mfma_f32_16x16x32_bf16 v[154:157], v[78:81], v[134:137], v[154:157]\n\t" \
    "v_mfma_f32_16x16x32_bf16 v[150:153], v[78:81], v[130:133], v[150:153]\n\t" \
    "s_waitcnt lgkmcnt(6)\n\t" \
    "v_mfma_f32_16x16x32_bf16 v[146:149], v[78:81], v[118:121], v[146:149]\n\t" \
    "s_waitcnt lgkmcnt(3)\n\t" \
    "v_mfma_f32_16x16x32_bf16 v[142:145], v[78:81], v[114:117], v[142:145]\n\t" \
    "ds_read_b64 v[78:79], %4 offset:3712\n\t" \
    "ds_read_b64 v[80:81], %4 offset:3720\n\t" \
    "ds_read_b128 v[134:137], %5 offset:3200\n\t" \
    "v_mfma_f32_16x16x32_bf16 v[154:157], v[82:85], v[130:133], v[154:157]\n\t" \
    "v_mfma_f32_16x16x32_bf16 v[150:153], v[82:85], v[126:129], v[150:153]\n\t" \
    "v_mfma_f32_16x16x32_bf16 v[146:149], v[82:85], v[114:117], v[146:149]\n\t" \
    "s_waitcnt lgkmcnt(3)\n\t" \
    "v_mfma_f32_16x16x32_bf16 v[142:145], v[82:85], v[110:113], v[142:145]\n\t" \
    "ds_read_b64 v[82:83], %4 offset:3200\n\t" \
    "ds_read_b64 v[84:85], %4 offset:3208\n\t" \
    "ds_read_b128 v[130:133], %5 offset:2688\n\t" \
    "v_mfma_f32_16x16x32_bf16 v[154:157], v[86:89], v[126:129], v[154:157]\n\t" \
    "v_mfma_f32_16x16x32_bf16 v[150:153], v[86:89], v[122:125], v[150:153]\n\t" \
    "v_mfma_f32_16x16x32_bf16 v[146:149], v[86:89], v[110:113], v[146:149]\n\t" \
    "ds_read_b64 v[86:87], %4 offset:2688\n\t" \
    "ds_read_b64 v[88:89], %4 offset:2696\n\t" \
    "ds_read_b128 v[126:129], %5 offset:2176\n\t" \
    "v_mfma_f32_16x16x32_bf16 v[154:157], v[90:93], v[122:125], v[154:157]\n\t" \
    "v_mfma_f32_16x16x32_bf16 v[150:153], v[90:93], v[118:121], v[150:153]\n\t" \
    "ds_read_b64 v[90:91], %4 offset:2176\n\t" \
    "ds_read_b64 v[92:93], %4 offset:2184\n\t" \
    "ds_read_b128 v[122:125], %5 offset:1664\n\t" \
    "v_mfma_f32_16x16x32_bf16 v[154:157], v[94:97], v[118:121], v[154:157]\n\t" \
    "v_mfma_f32_16x16x32_bf16 v[150:153], v[94:97], v[114:117], v[150:153]\n\t" \
    "ds_read_b64 v[94:95], %4 offset:1664\n\t" \
    "ds_read_b64 v[96:97], %4 offset:1672\n\t" \
    "ds_read_b128 v[118:121], %5 offset:1152\n\t" \
    "v_mfma_f32_16x16x32_bf16 v[154:157], v[98:101], v[114:117], v[154:157]\n\t" \
    "v_mfma_f32_16x16x32_bf16 v[150:153], v[98:101], v[110:113], v[150:153]\n\t" \
    "ds_read_b64 v[98:99], %4 offset:1152\n\t" \
    "ds_read_b64 v[100:101], %4 offset:1160\n\t" \
    "ds_read_b128 v[114:117], %5 offset:640\n\t" \
    "v_mfma_f32_16x16x32_bf16 v[154:157], v[102:105], v[110:113], v[154:157]\n\t" \
    "ds_read_b64 v[102:103], %4 offset:640\n\t" \
    "ds_read_b64 v[104:105], %4 offset:648\n\t" \
    "ds_read_b128 v[110:113], %5 offset:128\n\t" \
    "s_waitcnt lgkmcnt(15)\n\t" \
    "v_mfma_f32_16x16x32_bf16 v[154:157], v[78:81], v[134:137], v[154:157]\n\t" \
    "v_mfma_f32_16x16x32_bf16 v[150:153], v[78:81], v[130:133], v[150:153]\n\t" \
    "s_waitcnt lgkmcnt(6)\n\t" \
    "v_mfma_f32_16x16x32_bf16 v[146:149], v[78:81], v[118:121], v[146:149]\n\t" \
    "s_waitcnt lgkmcnt(3)\n\t" \
    "v_mfma_f32_16x16x32_bf16 v[142:145], v[78:81], v[114:117], v[142:145]\n\t" \
    "ds_read_b64 v[78:79], %4 offset:3648\n\t" \
    "ds_read_b64 v[80:81], %4 offset:3656\n\t" \
    "ds_read_b128 v[134:137], %5 offset:3136\n\t" \
    "v_mfma_f32_16x16x32_bf16 v[154:157], v[82:85], v[130:133], v[154:157]\n\t" \
    "v_mfma_f32_16x16x32_bf16 v[150:153], v[82:85], v[126:129], v[150:153]\n\t" \
    "v_mfma_f32_16x16x32_bf16 v[146:149], v[82:85], v[114:117], v[146:149]\n\t" \
    "s_waitcnt lgkmcnt(3)\n\t" \
    "v_mfma_f32_16x16x32_bf16 v[142:145], v[82:85], v[110:113], v[142:145]\n\t" \
    "ds_read_b64 v[82:83], %4 offset:3136\n\t" \
    "ds_read_b64 v[84:85], %4 offset:3144\n\t" \
    "ds_read_b128 v[130:133], %5 offset:2624\n\t" \
    "v_mfma_f32_16x16x32_bf16 v[154:157], v[86:89], v[126:129], v[154:157]\n\t" \
    "v_mfma_f32_16x16x32_bf16 v[150:153], v[86:89], v[122:125], v[150:153]\n\t" \
    "v_mfma_f32_16x16x32_bf16 v[146:149], v[86:89], v[110:113], v[146:149]\n\t" \
    "ds_read_b64 v[86:87], %4 offset:2624\n\t" \
    "ds_read_b64 v[88:89], %4 offset:2632\n\t" \
    "ds_read_b128 v[126:129], %5 offset:2112\n\t" \
    "v_mfma_f32_16x16x32_bf16 v[154:157], v[90:93], v[122:125], v[154:157]\n\t" \
    "v_mfma_f32_16x16x32_bf16 v[150:153], v[90:93], v[118:121], v[150:153]\n\t" \
    "ds_read_b64 v[90:91], %4 offset:2112\n\t" \
    "ds_read_b64 v[92:93], %4 offset:2120\n\t" \
    "ds_read_b128 v[122:125], %5 offset:1600\n\t" \
    "v_mfma_f32_16x16x32_bf16 v[154:157], v[94:97], v[118:121], v[154:157]\n\t" \
    "v_mfma_f32_16x16x32_bf16 v[150:153], v[94:97], v[114:117], v[150:153]\n\t" \
    "ds_read_b64 v[94:95], %4 offset:1600\n\t" \
    "ds_read_b64 v[96:97], %4 offset:1608\n\t" \
    "ds_read_b128 v[118:121], %5 offset:1088\n\t" \
    "v_mfma_f32_16x16x32_bf16 v[154:157], v[98:101], v[114:117], v[154:157]\n\t" \
    "v_mfma_f32_16x16x32_bf16 v[150:153], v[98:101], v[110:113], v[150:153]\n\t" \
    "ds_read_b64 v[98:99], %4 offset:1088\n\t" \
    "ds_read_b64 v[100:101], %4 offset:1096\n\t" \
    "ds_read_b128 v[114:117], %5 offset:576\n\t" \
    "v_mfma_f32_16x16x32_bf16 v[154:157], v[102:105], v[110:113], v[154:157]\n\t" \
    "ds_read_b64 v[102:103], %4 offset:576\n\t" \
    "ds_read_b64 v[104:105], %4 offset:584\n\t" \
    "ds_read_b128 v[110:113], %5 offset:64\n\t" \
    "s_waitcnt lgkmcnt(15)\n\t" \
    "v_mfma_f32_16x16x32_bf16 v[154:157], v[78:81], v[134:137], v[154:157]\n\t" \
    "v_mfma_f32_16x16x32_bf16 v[150:153], v[78:81], v[130:133], v[150:153]\n\t" \
    "s_waitcnt lgkmcnt(6)\n\t" \
    "v_mfma_f32_16x16x32_bf16 v[146:149], v[78:81], v[118:121], v[146:149]\n\t" \
    "s_waitcnt lgkmcnt(3)\n\t" \
    "v_mfma_f32_16x16x32_bf16 v[142:145], v[78:81], v[114:117], v[142:145]\n\t" \
    "ds_read_b64 v[78:79], %4 offset:3584\n\t" \
    "ds_read_b64 v[80:81], %4 offset:3592\n\t" \
    "ds_read_b128 v[134:137], %5 offset:3072\n\t" \
    "v_mfma_f32_16x16x32_bf16 v[154:157], v[82:85], v[130:133], v[154:157]\n\t" \
    "v_mfma_f32_16x16x32_bf16 v[150:153], v[82:85], v[126:129], v[150:153]\n\t" \
    "v_mfma_f32_16x16x32_bf16 v[146:149], v[82:85], v[114:117], v[146:149]\n\t" \
    "s_waitcnt lgkmcnt(3)\n\t" \
    "v_mfma_f32_16x16x32_bf16 v[142:145], v[82:85], v[110:113], v[142:145]\n\t" \
    "ds_read_b64 v[82:83], %4 offset:3072\n\t" \
    "ds_read_b64 v[84:85], %4 offset:3080\n\t" \
    "ds_read_b128 v[130:133], %5 offset:2560\n\t" \
    "v_mfma_f32_16x16x32_bf16 v[154:157], v[86:89], v[126:129], v[154:157]\n\t" \
    "v_mfma_f32_16x16x32_bf16 v[150:153], v[86:89], v[122:125], v[150:153]\n\t" \
    "v_mfma_f32_16x16x32_bf16 v[146:149], v[86:89], v[110:113], v[146:149]\n\t" \
    "ds_read_b64 v[86:87], %4 offset:2560\n\t" \
    "ds_read_b64 v[88:89], %4 offset:2568\n\t" \
    "ds_read_b128 v[126:129], %5 offset:2048\n\t" \
    "v_mfma_f32_16x16x32_bf16 v[154:157], v[90:93], v[122:125], v[154:157]\n\t" \
    "v_mfma_f32_16x16x32_bf16 v[150:153], v[90:93], v[118:121], v[150:153]\n\t" \
    "ds_read_b64 v[90:91], %4 offset:2048\n\t" \
    "ds_read_b64 v[92:93], %4 offset:2056\n\t" \
    "ds_read_b128 v[122:125], %5 offset:1536\n\t" \
    "v_mfma_f32_16x16x32_bf16 v[154:157], v[94:97], v[118:121], v[154:157]\n\t" \
    "v_mfma_f32_16x16x32_bf16 v[150:153], v[94:97], v[114:117], v[150:153]\n\t" \
    "ds_read_b64 v[94:95], %4 offset:1536\n\t" \
    "ds_read_b64 v[96:97], %4 offset:1544\n\t" \
    "ds_read_b128 v[118:121], %5 offset:1024\n\t" \
    "v_mfma_f32_16x16x32_bf16 v[154:157], v[98:101], v[114:117], v[154:157]\n\t" \
    "v_mfma_f32_16x16x32_bf16 v[150:153], v[98:101], v[110:113], v[150:153]\n\t" \
    "ds_read_b64 v[98:99], %4 offset:1024\n\t" \
    "ds_read_b64 v[100:101], %4 offset:1032\n\t" \
    "ds_read_b128 v[114:117], %5 offset:512\n\t" \
    "v_mfma_f32_16x16x32_bf16 v[154:157], v[102:105], v[110:113], v[154:157]\n\t" \
    "ds_read_b64 v[102:103], %4 offset:512\n\t" \
    "ds_read_b64 v[104:105], %4 offset:520\n\t" \
    "ds_read_b128 v[110:113], %5 offset:0\n\t" \
    "s_waitcnt lgkmcnt(15)\n\t" \
    "v_mfma_f32_16x16x32_bf16 v[154:157], v[78:81], v[134:137], v[154:157]\n\t" \
    "v_mfma_f32_16x16x32_bf16 v[150:153], v[78:81], v[130:133], v[150:153]\n\t" \
    "s_waitcnt lgkmcnt(6)\n\t" \
    "v_mfma_f32_16x16x32_bf16 v[146:149], v[78:81], v[118:121], v[146:149]\n\t" \
    "s_waitcnt lgkmcnt(3)\n\t" \
    "v_mfma_f32_16x16x32_bf16 v[142:145], v[78:81], v[114:117], v[142:145]\n\t" \
    "v_mfma_f32_16x16x32_bf16 v[154:157], v[82:85], v[130:133], v[154:157]\n\t" \
    "v_mfma_f32_16x16x32_bf16 v[150:153], v[82:85], v[126:129], v[150:153]\n\t" \
    "v_mfma_f32_16x16x32_bf16 v[146:149], v[82:85], v[114:117], v[146:149]\n\t" \
    "s_waitcnt lgkmcnt(0)\n\t" \
    "v_mfma_f32_16x16x32_bf16 v[142:145], v[82:85], v[110:113], v[142:145]\n\t" \
    "v_mfma_f32_16x16x32_bf16 v[154:157], v[86:89], v[126:129], v[154:157]\n\t" \
    "v_mfma_f32_16x16x32_bf16 v[150:153], v[86:89], v[122:125], v[150:153]\n\t" \
    "v_mfma_f32_16x16x32_bf16 v[146:149], v[86:89], v[110:113], v[146:149]\n\t" \
    "v_mfma_f32_16x16x32_bf16 v[154:157], v[90:93], v[122:125], v[154:157]\n\t" \
    "v_mfma_f32_16x16x32_bf16 v[150:153], v[90:93], v[118:121], v[150:153]\n\t" \
    "v_mfma_f32_16x16x32_bf16 v[154:157], v[94:97], v[118:121], v[154:157]\n\t" \
    "v_mfma_f32_16x16x32_bf16 v[150:153], v[94:97], v[114:117], v[150:153]\n\t" \
    "v_mfma_f32_16x16x32_bf16 v[154:157], v[98:101], v[114:117], v[154:157]\n\t" \
    "v_mfma_f32_16x16x32_bf16 v[150:153], v[98:101], v[110:113], v[150:153]\n\t" \
    "v_mfma_f32_16x16x32_bf16 v[154:157], v[102:105], v[110:113], v[154:157]\n\t"
template <int CS> DEVI void conv_mma(unsigned pa, unsigned pb, f32x4& a0, f32x4& a1, f32x4& a2, f32x4& a3) {
#define CONV_OPS : "=&{v[142:145]}"(a0), "=&{v[146:149]}"(a1), "=&{v[150:153]}"(a2), "=&{v[154:157]}"(a3) : "v"(pa), "v"(pb) \
    : "memory", CL8(78, 79, 80, 81, 82, 83, 84, 85), CL8(86, 87, 88, 89, 90, 91, 92, 93), CL8(94, 95, 96, 97, 98, 99, 100, 101), CL8(102, 103, 104, 105, 106, 107, 108, 109), \
      CL8(110, 111, 112, 113, 114, 115, 116, 117), CL8(118, 119, 120, 121, 122, 123, 124, 125), CL8(126, 127, 128, 129, 130, 131, 132, 133), CL8(134, 135, 136, 137, 138, 139, 140, 141)
    if (CS == 0) asm volatile(CONV_ASM_0 "s_nop 7" CONV_OPS); else asm volatile(CONV_ASM_1 "s_nop 7" CONV_OPS);
}

__global__ __launch_bounds__(512, 2) void fused_kernel(
    const unsigned short* __restrict__ x, const unsigned short* __restrict__ wqkv, const unsigned short* __restrict__ wo,
    const unsigned short* __restrict__ u, const unsigned short* __restrict__ lar, const unsigned short* __restrict__ aim,
    const unsigned short* __restrict__ cre, const unsigned short* __restrict__ cim, const unsigned short* __restrict__ ldt,
    const unsigned short* __restrict__ dsk, unsigned short* __restrict__ out, char* __restrict__ ws_) {
    extern __shared__ __attribute__((aligned(16))) char smem_[];
    const lptr lds = (lptr)smem_;
    GAS char* ws = (GAS char*)ws_;
    const int tid = threadIdx.x, lane = tid & 63, w = __builtin_amdgcn_readfirstlane(tid >> 6);
    const int r16 = lane & 15, g = lane >> 4, l31 = lane & 31, hh = lane >> 5;
    const int xcd = blockIdx.x & 7, sl = blockIdx.x >> 3, h = 4 * xcd + (sl >> 3), j = sl & 7, d0 = 64 * xcd + 2 * sl;
    const rsrc_t rws = mk_rsrc(ws_, WS_USED);
    STAMP_DECL;
    STAMP(0);

    const int kch = (w >> 1) & 1, kn = tid & 127;
    unsigned long long nonce; unsigned p_lar, p_aim, p_cre, p_cim, p_ldt; u32x4 ut;
    { const unsigned o = ((d0 + kch) * 128 + kn) * 2, o2 = (d0 + kch) * 2, o3 = (64 * sl + (tid >> 3)) * 1024 + xcd * 128 + (tid & 7) * 16;
      asm volatile("global_load_dwordx2 %0, %1, %2 sc1" : "=&v"(nonce) : "v"(0), "s"(ws_));
      asm volatile("buffer_load_dwordx4 %0, %1, %2, 0 offen" : "=&v"(ut) : "v"(o3), "s"(mk_rsrc(u, 2048 * 1024)));
      asm volatile("buffer_load_ushort %0, %1, %2, 0 offen" : "=&v"(p_lar) : "v"(o), "s"(mk_rsrc(lar, 131072)));
      asm volatile("buffer_load_ushort %0, %1, %2, 0 offen" : "=&v"(p_aim) : "v"(o), "s"(mk_rsrc(aim, 131072)));
      asm volatile("buffer_load_ushort %0, %1, %2, 0 offen" : "=&v"(p_cre) : "v"(o), "s"(mk_rsrc(cre, 131072)));
      asm volatile("buffer_load_ushort %0, %1, %2, 0 offen" : "=&v"(p_cim) : "v"(o), "s"(mk_rsrc(cim, 131072)));
      asm volatile("buffer_load_ushort %0, %1, %2, 0 offen" : "=&v"(p_ldt) : "v"(o2), "s"(mk_rsrc(ldt, 1024))); }
    unsigned long long tag;
    u32x4 ut0, ut1, pt0; u32x2 pt1;

    {
        const int hq = 4 * (xcd & 1) + (sl >> 3), rb = 8 * (xcd >> 1) + (sl & 7), koff = (sl & 7) + 2 * (sl >> 3);
        const rsrc_t rw = mk_rsrc(wqkv, 512 * 1536 * 2), rx = mk_rsrc(x, 2048 * 512 * 2);
        const unsigned lds0 = (unsigned)(uintptr_t)lds;
        const int rl = lane >> 3, c8 = lane & 7;
        const unsigned xv = rl * 1024 + ((c8 ^ rl) * 16);
        const unsigned wv = rl * 3072 + (((c8 >> 1) ^ (((rl >> 1) & 1) + 2 * (w & 1))) * 32) + (c8 & 1) * 16;
        auto issue = [&](int i) {
            const int kc = (i + koff) & 7; const unsigned dst = lds0 + L_RING + (i & 3) * SLOT + w * 1024;
            dma16(rx, dst, xv, (64 * rb + 8 * w) * 1024 + kc * 128);
            const unsigned ws0 = (64 * kc + 8 * w) * 3072 + hq * 128;
            dma16(rw, dst + 8192, wv, ws0); dma16(rw, dst + 16384, wv, ws0 + 1024); dma16(rw, dst + 24576, wv, ws0 + 2048); };
        issue(0); asm volatile("s_barrier" ::: "memory"); issue(1); issue(2);
        if (tid < 14) *(LAS unsigned*)(lds + L_MISC + (tid == 13 ? 32 : 64 + 4 * tid)) = 0u;
        asm volatile("s_waitcnt vmcnt(12)" : "+v"(nonce), "+v"(ut), "+v"(p_lar), "+v"(p_aim), "+v"(p_cre), "+v"(p_cim), "+v"(p_ldt) :: "memory");
        tag = mix64(nonce);
        sts16(lds + L_T1 + tid * 16, ut);
        f32x2 kc, ke, kf1;
        {
            const float dt = rbf(__builtin_amdgcn_exp2f(bf2f(p_ldt) * 1.4426950408889634f));
            const float zr = -rbf(__builtin_amdgcn_exp2f(bf2f(p_lar) * 1.4426950408889634f)) * dt, zi = bf2f(p_aim) * dt;
            const float zl = zr * 1.4426950408889634f, zt = zi * 0.15915494309189535f;
            auto cexp = [&](float p) { const float mag = __builtin_amdgcn_exp2f(zl * p), a = __builtin_amdgcn_fractf(zt * p);
                return (f32x2){mag * __builtin_amdgcn_cosf(a), mag * __builtin_amdgcn_sinf(a)}; };
            const f32x2 c = {2.f * bf2f(p_cre) * dt, 2.f * bf2f(p_cim) * dt}, s = cexp(w >= 4 ? 1536.f : 0.f);
            kc = (f32x2){c[0] * s[0] - c[1] * s[1], c[0] * s[1] + c[1] * s[0]};
            ke = cexp(32.f); kf1 = cexp(1.f);
            asm volatile("" : "+v"(kc), "+v"(ke), "+v"(kf1));
        }
        const int tg = w & 1, hl = w >> 1, hp = 4 * hq + hl;
        const int xa = (32 * tg + r16) * 128 + ((g ^ (r16 & 7)) * 16);
        const int wa = 8192 + (8 * g + (r16 >> 2)) * 128 + ((hl ^ (((r16 >> 3) & 1) + 2 * (g & 1))) * 32) + (r16 & 3) * 8;
        f32x4 aq[2] = {}, ak[2] = {}, av[2] = {};
        bf16x8 fa[10], fb[10];
        auto rd = [&](bf16x8 (&f)[10], int i) {
            const lptr sp = lds + L_RING + (i & 3) * SLOT;
#pragma unroll
            for (int ks = 0; ks < 2; ++ks) { const lptr wp = sp + wa + ks * 4096, xp = sp + (xa ^ (ks * 64));
                f[5 * ks] = tr_frag(wp); f[5 * ks + 1] = tr_frag(wp + 8192); f[5 * ks + 2] = tr_frag(wp + 16384);
                f[5 * ks + 3] = asb(lds16(xp)); f[5 * ks + 4] = asb(lds16(xp + 2048)); } };
        auto mm = [&](const bf16x8 (&f)[10]) {
#pragma unroll
            for (int ks = 0; ks < 2; ++ks)
#pragma unroll
                for (int tb = 0; tb < 2; ++tb) { const bf16x8 xb = f[5 * ks + 3 + tb];
                    aq[tb] = mfma16(f[5 * ks], xb, aq[tb]); ak[tb] = mfma16(f[5 * ks + 1], xb, ak[tb]); av[tb] = mfma16(xb, f[5 * ks + 2], av[tb]); } };
#define P1_STEP(F, FP, I) do { rd(F, I); mm(FP); if ((I) < 5) issue((I) + 3); } while (0)
        wait_vm<8>(); wg_barrier(); STAMP(1);
        rd(fa, 0); SBAR(); issue(3);
        { const lptr tp = lds + L_T1 + (8 * w + (r16 >> 2)) * 128 + g * 32 + (r16 & 3) * 8;
          sts16(lds + L_T2 + (16 * g + r16) * 128 + w * 16, asu(tr_frag(tp))); }
        wait_vm<8>(); wg_barrier(); STAMP(14);
        P1_STEP(fb, fa, 1);
        bst16_sc1(lds16(lds + L_T2 + tid * 16), rws, (tid >> 3) * 4096 + (tid & 7) * 16, WS_UT + xcd * 64 * 4096 + sl * 128);
        wait_vm<9>(); wg_barrier(); STAMP(15); P1_STEP(fa, fb, 2);
        wait_vm<9>(); wg_barrier(); STAMP(16); P1_STEP(fb, fa, 3);
        wait_vm<9>(); wg_barrier(); STAMP(17); P1_STEP(fa, fb, 4);
        wait_vm<8>(); wg_barrier(); STAMP(18); P1_STEP(fb, fa, 5);
        if (w == 0) { if (lane == 0) __hip_atomic_store((gu64*)(ws + WS_FLAG_U + (xcd * 32 + sl) * 8), tag, RLX_AGENT); }
        wait_vm<4>(); wg_barrier(); STAMP(19); P1_STEP(fa, fb, 6);
        wait_vm<0>(); wg_barrier(); STAMP(20);
        gu64* const fu = (gu64*)(ws + WS_FLAG_U + (xcd * 32 + l31) * 8);
        const unsigned long long fuv = peek(fu);
        P1_STEP(fb, fa, 7);
        mm(fb);
        STAMP(2);
        float q2 = 0.f, k2 = 0.f;
        u32x4 qw, kw, vw;
#pragma unroll
        for (int tb = 0; tb < 2; ++tb) {
            const f32x4 q = aq[tb] * QSCALE, k = ak[tb], v = av[tb];
            q2 = fmaxf(q2, q[0] * q[0] + q[1] * q[1] + q[2] * q[2] + q[3] * q[3]);
            k2 = fmaxf(k2, k[0] * k[0] + k[1] * k[1] + k[2] * k[2] + k[3] * k[3]);
            qw[2 * tb] = pk2(q[0], q[1]); qw[2 * tb + 1] = pk2(q[2], q[3]);
            kw[2 * tb] = pk2(k[0], k[1]); kw[2 * tb + 1] = pk2(k[2], k[3]);
            vw[2 * tb] = pk2(v[0], v[1]); vw[2 * tb + 1] = pk2(v[2], v[3]);
        }
        auto sw = [](u32x4 a) { unsigned a0 = a[0], a1 = a[1], b0 = a[2], b1 = a[3]; swap16(a0, b0); swap16(a1, b1); return (u32x4){a0, a1, b0, b1}; };
        kw = sw(kw); vw = sw(vw); qw = sw(qw);
        confirm(fuv, fu, tag);
        if (w < 4) {
            ut0 = bld16_sc1(rws, tid * 16, WS_UT + d0 * 4096); ut1 = bld16_sc1(rws, tid * 16, WS_UT + (d0 + 1) * 4096);
            pt0 = *(const u32x4*)(pos_tab.v + tid * 16); pt1 = *(const u32x2*)(pos_tab.v + tid * 16 + 8);
        }
        SBAR();
        const int tile = (hp * 8 + (rb >> 2)) * 8 + 2 * (rb & 3) + tg;
        const unsigned kvb = WS_QKV + tile * TS;
        const unsigned kpos = (g >> 1) * 512 + (16 * (g & 1) + r16) * 16;
        bst16_sc1(kw, rws, kpos, kvb + 1024);
        bst16_sc1(vw, rws, (g & 1) * 544 + (g >> 1) * 272 + r16 * 16, kvb + 2048);
        if (lane < 4) bst16_sc1((u32x4){0x3F803F80u, 0x3F803F80u, 0x3F803F80u, 0x3F803F80u}, rws, (lane & 1) * 544 + (lane >> 1) * 272 + 256, kvb + 2048);
        bst16_sc1(qw, rws, kpos, kvb);
        q2 = row_max(q2); k2 = row_max(k2);
        __builtin_amdgcn_raw_buffer_store_b32(__float_as_uint((r16 & 1) ? k2 : q2), rws, (r16 & 1) * 16 + g * 4, WS_NRM + tile * 32, AUX_SC1);
        wg_barrier();

        {
            const int role = w >> 2;
            const f32x2 er = {ke[0], ke[0]}, ei = {-ke[1], ke[1]};
            auto cmul = [](f32x2 a, f32x2 fr, f32x2 fi) { return a * fr + (f32x2){a[1], a[0]} * fi; };
            const lptr xp = lds + L_XY + kch * XY_CH + kn * 4;
            if (role == 0) {
                f32x2 a = kc;
#pragma unroll
                for (int i = 0; i < 48; ++i) { *(volatile LAS unsigned*)(xp + i * XY_ROW) = pk2(a[0], a[1]); a = cmul(a, er, ei); }
            } else {
                const f32x2 fr = {kf1[0], kf1[0]}, fi = {kf1[1], -kf1[1]};
                f32x2 a = kc, y = {1.f, 0.f};
#pragma unroll
                for (int i = 0; i < 32; ++i) {
                    if (i < 16) { *(volatile LAS unsigned*)(xp + (48 + i) * XY_ROW) = pk2(a[0], a[1]); a = cmul(a, er, ei); }
                    *(volatile LAS unsigned*)(xp + (64 + i) * XY_ROW) = pk2(y[0], y[1]); y = cmul(y, fr, fi);
                }
                const float sm = sum_x32(sum_x16(row_sum(a[0])));
                if (lane == 0) *(LAS float*)(lds + L_MISC + 48 + 4 * (w & 3)) = sm;
            }
        }
        STAMP(3);
        drain_vm();
        wg_barrier();
        if (tid == 0) __hip_atomic_store((gu64*)(ws + WS_FLAG_P + (hq * 32 + rb) * 128), tag, RLX_AGENT);
        STAMP(4);
    }

    KV kv; unsigned k2v;
    const lptr cnt = lds + L_MISC + 96;
    unsigned tb[8];
#pragma unroll
    for (int t = 0; t < 8; ++t) tb[t] = WS_QKV + ((h * 8 + w) * 8 + ((t + j) & 7)) * TS;
    const unsigned vk = lane * 16, vv = l31 <= 16 ? hh * 272 + l31 * 16 : 0x7fff0000u;
    const unsigned vn = ((lane >> 2) & 7) * 32 + 16 + (lane & 3) * 4, sn = WS_NRM + (h * 8 + w) * 256;
    if (w >= 4) {
        gu64* const fp = (gu64*)(ws + WS_FLAG_P + ((h >> 2) * 32 + (lane == 4 ? 4 * j + (w - 4) : 4 * w + (lane & 3))) * 128);
        wait_tag(fp, tag);
        STAMP(6);
        const unsigned qt = (h * 8 + j) * 8 + 2 * (w - 4);
        u32x4 q0, q1; unsigned q2v;
        kvq_request(kv, k2v, q0, q1, q2v, rws, vk, vv, vn, sn, ((lane >> 2) & 1) * 32 + (lane & 3) * 4, WS_NRM + qt * 32, WS_QKV + qt * TS, tb);
        sts16(lds + L_Q + 2 * (w - 4) * 1024 + lane * 16, q0); sts16(lds + L_Q + (2 * (w - 4) + 1) * 1024 + lane * 16, q1);
        float q2 = __uint_as_float(q2v);
        q2 = fmaxf(q2, dppf<0x121>(q2)); q2 = fmaxf(q2, dppf<0x122>(q2)); q2 = fmaxf(q2, dppf<0x124>(q2));
        if (lane == 0) *(LAS float*)(lds + L_MISC + 4 * (w - 4)) = q2;
        grp_arrive(cnt + 8, lane);
        STAMP(21); STAMP(22); STAMP(7);
        grp_wait(cnt, 4);
    } else {
        const int ch = w >> 1;
        gu64* const fp = (gu64*)(ws + WS_FLAG_P + ((h >> 2) * 32 + 4 * w + (lane & 3)) * 128);
        {
            const lptr xp = lds + L_XY + ch * XY_CH + (32 * (w & 1) + r16) * XY_ROW + g * 16;
            const lptr yp = lds + L_XY + ch * XY_CH + (64 + r16) * XY_ROW + g * 16;
            bf16x8 fx0[8], fx1[8], fy0[8], fy1[8];
#pragma unroll
            for (int ks = 0; ks < 8; ++ks) { fy0[ks] = asb(lds16(yp + ks * 64)); fx0[ks] = asb(lds16(xp + ks * 64));
                fy1[ks] = asb(lds16(yp + 16 * XY_ROW + ks * 64)); fx1[ks] = asb(lds16(xp + 16 * XY_ROW + ks * 64)); }
            SBAR();
            f32x4 a0 = {}, a1 = {}, a2 = {}, a3 = {};
#pragma unroll
            for (int ks = 0; ks < 8; ++ks) { a0 = mfma16(fy0[ks], fx0[ks], a0); a1 = mfma16(fy1[ks], fx0[ks], a1); a2 = mfma16(fy0[ks], fx1[ks], a2); a3 = mfma16(fy1[ks], fx1[ks], a3); }
            const lptr kp = lds + L_KD + ch * KD_CH + (32 * (w & 1) + r16) * 64 + g * 8;
            *(LAS u32x2*)kp = (u32x2){pk2(a0[0], a0[1]), pk2(a0[2], a0[3])};
            *(LAS u32x2*)(kp + 32) = (u32x2){pk2(a1[0], a1[1]), pk2(a1[2], a1[3])};
            *(LAS u32x2*)(kp + 1024) = (u32x2){pk2(a2[0], a2[1]), pk2(a2[2], a2[3])};
            *(LAS u32x2*)(kp + 1056) = (u32x2){pk2(a3[0], a3[1]), pk2(a3[2], a3[3])};
            if (lane == 0 && (w & 1) == 0) { const float s = *(LAS float*)(lds + L_MISC + 48 + 8 * ch) + *(LAS float*)(lds + L_MISC + 52 + 8 * ch);
                *(LAS unsigned*)(lds + L_KD + ch * KD_CH + 4096) = pk2(s, 0.f); }
            grp_arrive(cnt, lane);
        }
        const unsigned long long fpv = peek(fp);
        grp_wait(cnt, 4);
        STAMP(5);
        {
            unsigned kr[2][6];
#pragma unroll
            for (int c = 0; c < 2; ++c)
#pragma unroll
                for (int i = 0; i < 6; ++i) { const unsigned o = i < 4 ? pt0[i] : pt1[i - 4]; const lptr kp = lds + L_KD + c * KD_CH;
                    s16x2 v; v[0] = *(LAS short*)(kp + (o & 0xffffu)); v[1] = *(LAS short*)(kp + (o >> 16));
                    kr[c][i] = __builtin_bit_cast(unsigned, v); }
            SBAR();
#pragma unroll
            for (int c = 0; c < 2; ++c) {
                unsigned sft[5];
#pragma unroll
                for (int i = 0; i < 5; ++i) sft[i] = __builtin_amdgcn_alignbit(kr[c][i + 1], kr[c][i], 16);
                const lptr dp = lds + L_KR + c * KR_CH + tid * 16;
                sts16(dp, (u32x4){kr[c][0], kr[c][1], kr[c][2], kr[c][3]});
                sts16(dp + KR_CP, (u32x4){sft[0], sft[1], sft[2], sft[3]});
                sts16(dp + 2 * KR_CP, (u32x4){kr[c][1], kr[c][2], kr[c][3], kr[c][4]});
                sts16(dp + 3 * KR_CP, (u32x4){sft[1], sft[2], sft[3], sft[4]});
            }
            if (tid < 16) sts16(lds + L_KR + (tid >> 3) * KR_CH + ((tid >> 1) & 3) * KR_CP + 4096 + (tid & 1) * 16, (u32x4){0u, 0u, 0u, 0u});
            if (tid < 64) sts16(lds + L_U + (tid >> 5) * U_CH + (tid & 31) * 16, (u32x4){0u, 0u, 0u, 0u});
            sts16(lds + L_U + 512 + tid * 16, ut0); sts16(lds + L_U + U_CH + 512 + tid * 16, ut1);
            grp_arrive(cnt + 4, lane);
        }
        grp_wait(cnt + 4, 4);
        STAMP(6);
        const int cs = w & 1;
        const unsigned l0 = (unsigned)(uintptr_t)lds;
        const unsigned pa = l0 + L_KR + ch * KR_CH + (3 - (r16 & 3)) * KR_CP + 2 * (2044 - 4 * (r16 >> 2) - 16 * (g >> 1) + 8 * (g & 1)) - 64 * 7 - 3584;
        const unsigned pb = l0 + L_U + ch * U_CH + 512 + 32 * (r16 - (g >> 1)) + 16 * (g & 1) - 64 * 7;
        f32x4 a0, a1, a2, a3;
        STAMP(21);
        if (cs) conv_mma<1>(pa, pb, a0, a1, a2, a3); else conv_mma<0>(pa, pb, a0, a1, a2, a3);
        STAMP(22);
        const unsigned yv = r16 * 32 + g * 8, yb = WS_YT + (d0 + ch) * 4096;
        __builtin_amdgcn_raw_buffer_store_b64((u32x2){pk2(a0[0], a0[1]), pk2(a0[2], a0[3])}, rws, yv + (cs ? 1 : 0) * 512, yb, AUX_SC1);
        __builtin_amdgcn_raw_buffer_store_b64((u32x2){pk2(a1[0], a1[1]), pk2(a1[2], a1[3])}, rws, yv + (cs ? 2 : 3) * 512, yb, AUX_SC1);
        __builtin_amdgcn_raw_buffer_store_b64((u32x2){pk2(a2[0], a2[1]), pk2(a2[2], a2[3])}, rws, yv + (cs ? 5 : 4) * 512, yb, AUX_SC1);
        __builtin_amdgcn_raw_buffer_store_b64((u32x2){pk2(a3[0], a3[1]), pk2(a3[2], a3[3])}, rws, yv + (cs ? 6 : 7) * 512, yb, AUX_SC1);
        grp_arrive(cnt + 12, lane);
        drain_vm();
        if (lane == 0) { const unsigned n = __hip_atomic_fetch_add((LAS unsigned*)(cnt + 16), 1u, __ATOMIC_RELAXED, __HIP_MEMORY_SCOPE_WORKGROUP);
            if (n == 3) __hip_atomic_store((gu64*)(ws + WS_FLAG_Y + (xcd * 32 + sl) * 8), tag, RLX_AGENT); }
        confirm(fpv, fp, tag);
        kv_request(kv, k2v, rws, vk, vv, vn, sn, tb);
        grp_wait(cnt + 8, 4);
        grp_wait(cnt + 12, 4);
        STAMP(7);
    }
    STAMP(8);

    {
        if (w >= 4) grp_wait(cnt + 8, 4);
        float q2 = *(LAS float*)(lds + L_MISC + 4 * (lane & 3)), k2 = __uint_as_float(k2v);
        q2 = fmaxf(q2, dppf<0x122>(q2)); q2 = fmaxf(q2, dppf<0x121>(q2));
        k2 = max_x16(row_max(k2));
        const bool fast = __builtin_amdgcn_readfirstlane((q2 * k2 * (16.f * 1.05f) <= SAFE2) ? 1 : 0) != 0;
        const lptr qp = lds + L_Q + lane * 16;
        const int pbase = (w >= 4 ? L_PB : L_PA) + (w & 3) * P_SZ;
        const lptr pp = lds + pbase;
        STAMP(9);
        int shifted = fast ? 0 : 1;
        if (__builtin_expect(fast, 1)) {
            const unsigned l0 = (unsigned)(uintptr_t)lds;
            attn_fast(kv, l0 + L_Q + lane * 16, l0 + pbase + lane * 16, l0 + pbase + lane * 4);
        }
        asm volatile("" : "+v"(shifted));
        if (__builtin_expect(__builtin_amdgcn_readfirstlane(shifted), 0)) {
            kv_landed(kv);
            if (lane == 0) { *(LAS unsigned*)(lds + L_MISC + 32) = 1u; *(LAS unsigned*)(lds + L_MISC + 64 + 4 * w) = 1u; }
#pragma unroll 1
            for (int qb = 0; qb < 8; ++qb) {
                f32x16 o; float m;
                attn_block_shifted(rws, WS_QKV + (h * 8 + w) * 8 * TS, vk, vv, asb(lds16(qp + qb * 1024)), o, m);
                put_partial(pp + qb * (4 * P_SZ), lane, o, m);
            }
        }
        STAMP(10);
        if (w < 4) {
            wait_tag((gu64*)(ws + WS_FLAG_Y + (xcd * 32 + l31) * 8), tag);
            const unsigned yb = WS_YT + (64 * xcd + 16 * w) * 4096 + sl * 128;
            unsigned yy[8];
#pragma unroll
            for (int i = 0; i < 8; ++i) { s16x2 v;
                v[0] = (short)__builtin_amdgcn_raw_buffer_load_b16(rws, lane * 2 + (2 * i) * 4096, yb, AUX_SC1);
                v[1] = (short)__builtin_amdgcn_raw_buffer_load_b16(rws, lane * 2 + (2 * i + 1) * 4096, yb, AUX_SC1);
                yy[i] = __builtin_bit_cast(unsigned, v); }
            const rsrc_t ru = mk_rsrc(u, 2048 * 1024), rd = mk_rsrc(dsk, 1024);
            const unsigned uo = (64 * sl + lane) * 1024, co = (64 * xcd + 16 * w) * 2;
            const u32x4 ua = bld16(ru, uo, co), ub = bld16(ru, uo + 16, co), da = bld16(rd, 0, co), db = bld16(rd, 16, co);
            SBAR();
            u32x4 ra, rb;
#pragma unroll
            for (int i = 0; i < 4; ++i) {
                const unsigned us = pk2(bf_lo(ua[i]) * bf_lo(da[i]), bf_hi(ua[i]) * bf_hi(da[i])), vs = pk2(bf_lo(ub[i]) * bf_lo(db[i]), bf_hi(ub[i]) * bf_hi(db[i]));
                ra[i] = pk2(bf_lo(yy[i]) + bf_lo(us), bf_hi(yy[i]) + bf_hi(us)); rb[i] = pk2(bf_lo(yy[4 + i]) + bf_lo(vs), bf_hi(yy[4 + i]) + bf_hi(vs)); }
            u32x4* op = (u32x4*)(out + (64 * sl + lane) * 1024 + 512 + 64 * xcd + 16 * w);
            op[0] = ra; op[1] = rb;
        }
        wg_barrier();
        const lptr pA = lds + L_PA + w * (4 * P_SZ), pB = lds + L_PB + w * (4 * P_SZ);
        auto part = [&](int i) { return (i >= 4 ? pB : pA) + (i & 3) * P_SZ; };
        f32x4 oa = {}, ob = {}; float l = 0.f;
        if (__builtin_expect(__builtin_amdgcn_readfirstlane(*(LAS unsigned*)(lds + L_MISC + 32)) == 0u, 1)) {
            f32x4 ra[8], rb[8]; float rl_[8];
#pragma unroll
            for (int i = 0; i < 8; ++i) { ra[i] = *(LAS f32x4*)(part(i) + lane * 16); rb[i] = *(LAS f32x4*)(part(i) + 1024 + lane * 16);
                rl_[i] = *(LAS float*)(part(i) + 2048 + l31 * 4); }
            SBAR();
            oa = ((ra[0] + ra[1]) + (ra[2] + ra[3])) + ((ra[4] + ra[5]) + (ra[6] + ra[7]));
            ob = ((rb[0] + rb[1]) + (rb[2] + rb[3])) + ((rb[4] + rb[5]) + (rb[6] + rb[7]));
            l = ((rl_[0] + rl_[1]) + (rl_[2] + rl_[3])) + ((rl_[4] + rl_[5]) + (rl_[6] + rl_[7]));
        } else {
            float mi[8], mx = -INFINITY;
#pragma unroll
            for (int i = 0; i < 8; ++i) { mi[i] = *(LAS unsigned*)(lds + L_MISC + 64 + 4 * i) ? *(LAS float*)(part(i) + 2048 + 128 + l31 * 4) : 0.f; mx = fmaxf(mx, mi[i]); }
#pragma unroll
            for (int i = 0; i < 8; ++i) { const float f = __builtin_amdgcn_exp2f(mi[i] - mx);
                oa += *(LAS f32x4*)(part(i) + lane * 16) * f; ob += *(LAS f32x4*)(part(i) + 1024 + lane * 16) * f;
                l += *(LAS float*)(part(i) + 2048 + l31 * 4) * f; }
        }
        const float rl = 1.f / l;
        oa *= rl; ob *= rl;
        unsigned a0 = pk2(oa[0], oa[1]), a1 = pk2(oa[2], oa[3]), b0 = pk2(ob[0], ob[1]), b1 = pk2(ob[2], ob[3]);
        swap32(a0, b0); swap32(a1, b1);
        const u32x4 ow = {a0, a1, b0, b1};
        bst16_sc1(ow, rws, (l31 >> 4) * 16384 + hh * 256 + (l31 & 15) * 16, WS_O + (16 * j + 2 * w) * 16384 + h * 512);
        drain_vm();
        wg_barrier();
        if (tid == 0) __hip_atomic_store((gu64*)(ws + WS_FLAG_O + (j * 32 + h) * 8), tag, RLX_AGENT);
        STAMP(11);
    }

    {
        const int ncol0 = 256 * (xcd >> 2) + 64 * (h & 3);
        const unsigned l0 = (unsigned)(uintptr_t)lds;
        const int lane = __builtin_amdgcn_mbcnt_hi(~0u, __builtin_amdgcn_mbcnt_lo(~0u, 0u)), r16 = lane & 15, g = lane >> 4, l31 = lane & 31;
        const int rl = lane >> 3, c8 = lane & 7;
        { const rsrc_t rwo = mk_rsrc(wo, 512 * 512 * 2);
          const unsigned v0 = rl * 1024 + (((c8 >> 1) ^ ((rl >> 1) & 1)) * 32) + (c8 & 1) * 16, v1 = v0 ^ 64;
#pragma unroll
          for (int i = 0; i < 8; ++i) dma16(rwo, l0 + L_WO + w * 8192 + i * 1024, (i & 1) ? v1 : v0, (64 * w + 8 * i) * 1024 + ncol0 * 2); }
        if (w == 0) wait_tag((gu64*)(ws + WS_FLAG_O + (j * 32 + l31) * 8), tag);
        drain_vm();
        wg_barrier();
        STAMP(12);
        if (blockIdx.x == 0 && tid == 0) __hip_atomic_store((gu64*)(ws + WS_NONCE), tag, RLX_AGENT);
        const int rgp = w & 3, kh = w >> 2, row0 = 256 * j + 64 * (xcd & 3) + 16 * rgp;
        u32x4 of[8];
#pragma unroll
        for (int ks = 0; ks < 8; ++ks) of[ks] = bld16_sc1(rws, lane * 16 + ks * 1024, WS_O + (row0 >> 4) * 16384 + kh * 8192);
        SBAR();
        f32x4 pr[4] = {};
        const int fk = ((r16 >> 3) & 1) + 2 * (g & 1);
        lptr wp[4];
#pragma unroll
        for (int nb = 0; nb < 4; ++nb) wp[nb] = lds + L_WO + kh * 32768 + (8 * g + (r16 >> 2)) * 128 + ((nb ^ (2 * kh) ^ fk) * 32) + (r16 & 3) * 8;
        bf16x8 wf[8][4];
#pragma unroll
        for (int ks = 0; ks < 8; ++ks)
#pragma unroll
            for (int nb = 0; nb < 4; ++nb) wf[ks][nb] = tr_frag(wp[nb] + ks * 4096);
        SBAR();
#pragma unroll
        for (int ks = 0; ks < 8; ++ks)
#pragma unroll
            for (int nb = 0; nb < 4; ++nb) pr[nb] = mfma16(wf[ks][nb], asb(of[ks]), pr[nb]);
        const lptr xo = lds + L_PX + w * 2048 + lane * 16, xi = lds + L_PX + (w ^ 4) * 2048 + lane * 16;
        *(LAS f32x4*)xo = pr[2]; *(LAS f32x4*)(xo + 1024) = pr[3];
        wg_barrier();
        const f32x4 p0 = pr[0] + *(LAS f32x4*)xi, p1 = pr[1] + *(LAS f32x4*)(xi + 1024);
        unsigned a0 = pk2(p0[0], p0[1]), a1 = pk2(p0[2], p0[3]), b0 = pk2(p1[0], p1[1]), b1 = pk2(p1[2], p1[3]);
        swap16(a0, b0); swap16(a1, b1);
        *(u32x4*)(out + (row0 + r16) * 1024 + ncol0 + 32 * kh + 16 * (g & 1) + 8 * (g >> 1)) = (u32x4){a0, a1, b0, b1};
        STAMP(13);
        STAMP_FLUSH;
    }
}

extern "C" void kernel_launch(void* const* d_in, const int* in_sizes, int n_in, void* d_out, int out_size, void* d_ws, size_t ws_size, hipStream_t stream) {
    (void)in_sizes; (void)n_in; (void)out_size; (void)ws_size;
    (void)hipFuncSetAttribute((const void*)fused_kernel, hipFuncAttributeMaxDynamicSharedMemorySize, LDS_BYTES);
    typedef const unsigned short* cp;
    fused_kernel<<<256, 512, LDS_BYTES, stream>>>((cp)d_in[0], (cp)d_in[1], (cp)d_in[2], (cp)d_in[3], (cp)d_in[4], (cp)d_in[5], (cp)d_in[6], (cp)d_in[7],
                                                  (cp)d_in[8], (cp)d_in[9], (unsigned short*)d_out, (char*)d_ws);
}
```
